```python
import torch

def _softmax(x, axis=-1):
    return torch.softmax(x, dim=axis)
def _gelu(x):
    return torch.nn.functional.gelu(x, approximate="tanh")
def _silu(x):
    return torch.nn.functional.silu(x)
def _sigmoid(x):
    return torch.sigmoid(x)
def _layernorm(x, axis=-1, eps=1e-5):
    m = x.mean(dim=axis, keepdim=True)
    v = x.var(dim=axis, keepdim=True, unbiased=False)
    return (x - m) / torch.sqrt(v + eps)
def _scatter_add(tgt, idx, src, axis=0):
    return tgt.clone().index_add_(axis, idx, src)
def _segment_sum(src, seg, n):
    out = torch.zeros((n,) + src.shape[1:], dtype=src.dtype)
    return out.index_add_(0, seg, src)
def _pad(x, pw):
    flat = tuple(v for pair in reversed(pw) for v in pair)
    return torch.nn.functional.pad(x, flat)

kernel_name = 'parallel_sum__sliding_block_cached__glu_gated_swiglu__decode'

def setup_inputs():

    _w = (lambda a: (lambda t: t.to(torch.bfloat16) if t.is_floating_point() else t)(torch.as_tensor(a)))
    def _A_setup():
        B, Sq, D = 1, 1, 512
        Hq, Hkv, Sk, Dh = 32, 8, 65536, 128
        assert Hq % Hkv == 0, (Hq, Hkv)
        return {
            "x": _w((lambda *s: torch.randn(*s, dtype=torch.bfloat16) * 0.05)(B, Sq, D)),
            "k_cache": _w((lambda *s: torch.randn(*s, dtype=torch.bfloat16) * 1.6)(B, Hkv, Sk, Dh)),
            "v_cache": _w((lambda *s: torch.randn(*s, dtype=torch.bfloat16) * 0.8)(B, Hkv, Sk, Dh)),
            "wq": _w((lambda *s: torch.randn(*s, dtype=torch.bfloat16) * 1.6)(D, Hq * Dh) * (D ** -0.5)),
            "wo": _w((lambda *s: torch.randn(*s, dtype=torch.bfloat16) * 0.8)(Hq * Dh, D) * ((Hq * Dh) ** -0.5)),
        }
    _A_in = _A_setup()

    def _B_setup():
        B, S, D, F = 1, 1, 512, 1792
        return {
            "x": _w((lambda *s: torch.randn(*s, dtype=torch.bfloat16) * 0.05)(B, S, D)),
            "w1": _w((lambda *s: torch.randn(*s, dtype=torch.bfloat16) * 0.4)(D, F) * (2.0 / D) ** 0.5),
            "w3": _w((lambda *s: torch.randn(*s, dtype=torch.bfloat16) * 0.4)(D, F) * (2.0 / D) ** 0.5),
            "w2": _w((lambda *s: torch.randn(*s, dtype=torch.bfloat16) * 0.4)(F, D) * (2.0 / F) ** 0.5),
        }
    _B_in = _B_setup()

    return {
        "A_x": _A_in["x"],
        "A_k_cache": _A_in["k_cache"],
        "A_v_cache": _A_in["v_cache"],
        "A_wq": _A_in["wq"],
        "A_wo": _A_in["wo"],
        "B_x": _B_in["x"],
        "B_w1": _B_in["w1"],
        "B_w3": _B_in["w3"],
        "B_w2": _B_in["w2"],
    }

def reference(A_x, A_k_cache, A_v_cache, A_wq, A_wo, B_x, B_w1, B_w3, B_w2):
    def _A_ref(x, k_cache, v_cache, wq, wo):
        B, Sq, D = x.shape
        Hq, Dh = 32, 128
        Hkv, Sk = k_cache.shape[1], k_cache.shape[2]
        W = min(4096, max(256, Sk // 4))
        rep = Hq // Hkv
        q = (lambda x, *d: x.permute(*d))((x @ wq).reshape(B, Sq, Hq, Dh), 0, 2, 1, 3)
        k = (lambda x, n, axis: torch.repeat_interleave(x, n, dim=axis))(k_cache[:, :, Sk - W:, :], rep, axis=1)
        v = (lambda x, n, axis: torch.repeat_interleave(x, n, dim=axis))(v_cache[:, :, Sk - W:, :], rep, axis=1)
        scores = torch.einsum("bhqd,bhkd->bhqk", q, k) * (Dh ** -0.5)
        p = _softmax(scores, axis=-1)
        o = torch.einsum("bhqk,bhkd->bhqd", p, v)
        return (lambda x, *d: x.permute(*d))(o, 0, 2, 1, 3).reshape(B, Sq, Hq * Dh) @ wo

    def _B_ref(x, w1, w3, w2):
        gate = torch.einsum("bsd,df->bsf", x, w1)
        up = torch.einsum("bsd,df->bsf", x, w3)
        h = _silu(gate) * up
        out = torch.einsum("bsf,fd->bsd", h, w2)
        return out

    x = A_x
    xn = _layernorm(x, axis=-1)
    return x + _A_ref(xn, A_k_cache, A_v_cache, A_wq, A_wo) + _B_ref(xn, B_w1, B_w3, B_w2)
```

```cpp
#include <hip/hip_runtime.h>
#include <hip/hip_bf16.h>
#include <stdint.h>

typedef unsigned short u16;
typedef unsigned u32x4 __attribute__((ext_vector_type(4)));
__device__ __forceinline__ float bf2f(u16 v){ return __uint_as_float(((unsigned)v)<<16); }
__device__ __forceinline__ float rbf(float f){ __hip_bfloat16 b = __float2bfloat16(f); return __bfloat162float(b); }

constexpr int D=512, HQ=32, HKV=8, SK=65536, DH=128, W=4096, F=1792;

__device__ void ln(const u16* x, float* xn, float* red){
  int t=threadIdx.x;
  float a=bf2f(x[t]), b=bf2f(x[t+256]);
  float s=a+b;
  for(int o=32;o;o>>=1) s+=__shfl_xor(s,o);
  if((t&63)==0) red[t>>6]=s;
  __syncthreads();
  float m=(red[0]+red[1]+red[2]+red[3])*(1.f/512);
  __syncthreads();
  float da=a-m, db=b-m;
  float v=da*da+db*db;
  for(int o=32;o;o>>=1) v+=__shfl_xor(v,o);
  if((t&63)==0) red[t>>6]=v;
  __syncthreads();
  float var=(red[0]+red[1]+red[2]+red[3])*(1.f/512);
  float r=rsqrtf(var+1e-5f);
  xn[t]=rbf(da*r); xn[t+256]=rbf(db*r);
  __syncthreads();
}

__global__ void k1(const u16* x,const u16* wq,const u16* w1,const u16* w3,float* q,float* h){
  __shared__ float xn[512]; __shared__ float red[4];
  ln(x,xn,red);
  int c=blockIdx.x*256+threadIdx.x;
  if(c<4096){ float a=0; for(int k=0;k<512;k++) a+=xn[k]*bf2f(wq[k*4096+c]); q[c]=rbf(a);}
  else { c-=4096; if(c<F){ float g=0,u=0; for(int k=0;k<512;k++){ g+=xn[k]*bf2f(w1[k*F+c]); u+=xn[k]*bf2f(w3[k*F+c]);} g=rbf(g);u=rbf(u); float s=rbf(g/(1.f+__expf(-g))); h[c]=rbf(s*u);} }
}
__global__ void k2(const u16* K,const u16* V,const float* q,float* po,float* pml){
  int s=blockIdx.x, hq=blockIdx.y, kv=hq/4, t=threadIdx.x;
  __shared__ float qs[128], p[128], red[2];
  qs[t]=q[hq*128+t]; __syncthreads();
  const u16* kr=K+((size_t)kv*SK+SK-W+s*128+t)*DH;
  float a=0; for(int d=0;d<128;d++) a+=qs[d]*bf2f(kr[d]);
  a*=0.08838834764831845f;
  float m=a; for(int o=32;o;o>>=1) m=fmaxf(m,__shfl_xor(m,o));
  if((t&63)==0) red[t>>6]=m; __syncthreads(); m=fmaxf(red[0],red[1]); __syncthreads();
  float e=__expf(a-m); p[t]=e; float l=e; for(int o=32;o;o>>=1) l+=__shfl_xor(l,o);
  if((t&63)==0) red[t>>6]=l; __syncthreads(); l=red[0]+red[1];
  const u16* vr=V+((size_t)kv*SK+SK-W+s*128)*DH;
  float o=0; for(int k=0;k<128;k++) o+=p[k]*bf2f(vr[k*DH+t]);
  po[(hq*32+s)*128+t]=o; if(t==0){pml[(hq*32+s)*2]=m; pml[(hq*32+s)*2+1]=l;}
}
__global__ void k3(const float* po,const float* pml,const float* h,const u16* wo,const u16* w2,float* part){
  int b=blockIdx.x,t=threadIdx.x;
  __shared__ float o[128];
  const u16* w;
  if(b<32){
    if(t<128){ float M=-1e30f; for(int s=0;s<32;s++) M=fmaxf(M,pml[(b*32+s)*2]); float L=0,a=0; for(int s=0;s<32;s++){ float e=__expf(pml[(b*32+s)*2]-M); L+=e*pml[(b*32+s)*2+1]; a+=e*po[(b*32+s)*128+t];} o[t]=rbf(a/L);}
    w=wo+(size_t)b*128*512;
  } else { if(t<128) o[t]=h[(b-32)*128+t]; w=w2+(size_t)(b-32)*128*512; }
  __syncthreads();
  float a=0; for(int k=0;k<128;k++) a+=o[k]*bf2f(w[k*512+t]);
  part[b*512+t]=a;
}
__global__ void k4(const u16* x,const float* part,__hip_bfloat16* out){
  int t=threadIdx.x; float a=0,f=0; for(int b=0;b<32;b++) a+=part[b*512+t]; for(int b=32;b<46;b++) f+=part[b*512+t];
  out[t]=__float2bfloat16(bf2f(x[t])+a+f);
}

extern "C" void kernel_launch(void* const* d_in, const int* in_sizes, int n_in,
                              void* d_out, int out_size, void* d_ws, size_t ws_size,
                              hipStream_t stream) {
  const u16* x=(const u16*)d_in[0]; const u16* K=(const u16*)d_in[1]; const u16* V=(const u16*)d_in[2];
  const u16* wq=(const u16*)d_in[3]; const u16* wo=(const u16*)d_in[4];
  const u16* w1=(const u16*)d_in[6]; const u16* w3=(const u16*)d_in[7]; const u16* w2=(const u16*)d_in[8];
  float* ws=(float*)d_ws; float* q=ws; float* h=ws+4096; float* po=ws+8192; float* pml=po+32*32*128; float* part=pml+32*32*2;
  k1<<<23,256,0,stream>>>(x,wq,w1,w3,q,h);
  k2<<<dim3(32,32),128,0,stream>>>(K,V,q,po,pml);
  k3<<<46,512,0,stream>>>(po,pml,h,wo,w2,part);
  k4<<<1,512,0,stream>>>(x,part,(__hip_bfloat16*)d_out);
}
```

```cpp
#include <hip/hip_runtime.h>
#include <hip/hip_bf16.h>
#include <stdint.h>

typedef unsigned short u16;
typedef unsigned u32;
typedef unsigned long long u64;
typedef u32 u32x4 __attribute__((ext_vector_type(4)));
typedef u32 u32x2 __attribute__((ext_vector_type(2)));
typedef float f32x4 __attribute__((ext_vector_type(4)));
typedef float f32x2 __attribute__((ext_vector_type(2)));
typedef __bf16 bf16x8 __attribute__((ext_vector_type(8)));
#define LAS __attribute__((address_space(3)))
#define GAS __attribute__((address_space(1)))

#define DIAG 0

namespace {
constexpr int DM = 512, HQ = 32, HKV = 8, DH = 128, WIN = 4096, FF = 1792;
constexpr int NWG = 256, NTHR = 576;
constexpr u32 WS_EPOCH = 0;
constexpr u32 WS_QS = 4096;
constexpr u32 WS_ML = WS_QS + 8 * 4 * 512 * 8;
constexpr u32 WS_GS = WS_ML + 8 * 4 * 32 * 16;
constexpr u32 WS_OS = WS_GS + 224 * 8 * 8 * 16;
constexpr u32 WS_PS = WS_OS + 8 * 32 * 32 * 128;
constexpr u32 WS_END = WS_PS + 256 * 256 * 16;
constexpr u32 WS_DIAG = 8u << 20;
constexpr u32 SPIN_CAP = 1u << 22;

struct Params {
  const u16 *x, *K, *V, *wq, *wo, *w1, *w3, *w2;
  u16* out;
  unsigned char* ws;
  u64 kv_head_stride;
  u64 kv_win_off;
};

__device__ __forceinline__ float bf_lo(u32 v) { return __uint_as_float(v << 16); }
__device__ __forceinline__ float bf_hi(u32 v) { return __uint_as_float(v & 0xffff0000u); }
__device__ __forceinline__ u32 pk_bf16(float lo, float hi) {
  typedef __bf16 bf16x2 __attribute__((ext_vector_type(2)));
  bf16x2 r = {(__bf16)lo, (__bf16)hi};
  return __builtin_bit_cast(u32, r);
}
__device__ __forceinline__ float rbf(float f) { return (float)(__bf16)f; }
template <int CTRL> __device__ __forceinline__ float dpp(float x) {
  return __builtin_bit_cast(float, __builtin_amdgcn_mov_dpp(__builtin_bit_cast(int, x), CTRL, 0xf, 0xf, true));
}
constexpr int XOR1 = 0xB1, XOR2 = 0x4E, XOR7 = 0x141, XOR8 = 0x128, ROR4 = 0x124;
__device__ __forceinline__ float tr32(float a, float b) {
  auto s = __builtin_amdgcn_permlane32_swap(__float_as_uint(a), __float_as_uint(b), false, false);
  return __uint_as_float(s[0]) + __uint_as_float(s[1]);
}
__device__ __forceinline__ float tr16(float a, float b) {
  auto s = __builtin_amdgcn_permlane16_swap(__float_as_uint(a), __float_as_uint(b), false, false);
  return __uint_as_float(s[0]) + __uint_as_float(s[1]);
}
__device__ __forceinline__ float sum32(float x) { return tr32(x, x); }
__device__ __forceinline__ float sum16(float x) { return tr16(x, x); }
__device__ __forceinline__ float max32(float x) {
  auto s = __builtin_amdgcn_permlane32_swap(__float_as_uint(x), __float_as_uint(x), false, false);
  return fmaxf(__uint_as_float(s[0]), __uint_as_float(s[1]));
}
__device__ __forceinline__ float max16(float x) {
  auto s = __builtin_amdgcn_permlane16_swap(__float_as_uint(x), __float_as_uint(x), false, false);
  return fmaxf(__uint_as_float(s[0]), __uint_as_float(s[1]));
}
__device__ __forceinline__ float wave_sum(float x) {
  x += dpp<XOR1>(x); x += dpp<XOR2>(x); x += dpp<XOR7>(x); x += dpp<XOR8>(x);
  return sum32(sum16(x));
}
__device__ __forceinline__ u32x4 ldnt(const void* p) { return __builtin_nontemporal_load((const u32x4*)p); }
#define CBAR() asm volatile("" ::: "memory")
#define SB() __builtin_amdgcn_sched_barrier(0)

#define STAMP(i) do {} while (0)
#define STAMP_DECL do {} while (0)
#define STAMP_FLUSH() do {} while (0)

struct Lds {
  float qp[4][64];
  float gp[4][128];
  u32 qb[256];
  float op[4][512];
  float ml[4][4][2];
  float coef[24];
  u32 tag;
  u32 f_qp, f_gp, f_q, f_op, f_coef, f_visd, f_tag;
};

__device__ __forceinline__ void lds_wait(volatile LAS u32* f, u32 want) {
#pragma unroll 1
  for (u32 s = 0; s < SPIN_CAP; ++s) {
    if (*f >= want) break;
    __builtin_amdgcn_s_sleep(1);
  }
  CBAR();
}
__device__ __forceinline__ void lds_post(LAS u32* f, int lane) {
  CBAR();
  if (lane == 0) __hip_atomic_fetch_add(f, 1u, __ATOMIC_RELAXED, __HIP_MEMORY_SCOPE_WORKGROUP);
  CBAR();
}

__device__ __forceinline__ void ln_stats(u32x4 xv, float& m, float& sd) {
  float e[8];
#pragma unroll
  for (int k = 0; k < 4; ++k) { e[2 * k] = bf_lo(xv[k]); e[2 * k + 1] = bf_hi(xv[k]); }
  float s = ((e[0] + e[1]) + (e[2] + e[3])) + ((e[4] + e[5]) + (e[6] + e[7]));
  const float mean = wave_sum(s) * (1.f / DM);
  float v = 0.f;
#pragma unroll
  for (int k = 0; k < 8; ++k) { const float d = e[k] - mean; v = fmaf(d, d, v); }
  const float var = wave_sum(v) * (1.f / DM);
  m = rbf(mean);
  sd = rbf(sqrtf(rbf(rbf(var) + 1e-5f)));
}
__device__ __forceinline__ float ln_apply(u16 xr, float m, float sd) {
  return rbf(rbf(__uint_as_float((u32)xr << 16) - m) / sd);
}
__device__ __forceinline__ float tred8(const float (&a)[8], int lane) {
  const float b0 = tr32(a[0], a[4]), b1 = tr32(a[1], a[5]), b2 = tr32(a[2], a[6]), b3 = tr32(a[3], a[7]);
  const float c0 = tr16(b0, b2), c1 = tr16(b1, b3);
  const float d0 = c0 + dpp<XOR8>(c0), d1 = c1 + dpp<XOR8>(c1);
  return (lane & 8) ? d1 : d0;
}

__global__ void __launch_bounds__(NTHR) fused_decode(Params p) {
  __shared__ __attribute__((aligned(16))) Lds sm_;
  LAS Lds* sm = (LAS Lds*)&sm_;
  const int b = blockIdx.x, tid = threadIdx.x, lane = tid & 63;
  const int wave = __builtin_amdgcn_readfirstlane(tid >> 6);
  const int h = b & 7, j = b >> 3;
  const __amdgpu_buffer_rsrc_t wsr = __builtin_amdgcn_make_buffer_rsrc(p.ws, 0, WS_END, 0x00020000);
  STAMP_DECL;
  STAMP(0);

  if (wave < 4) {
    const int w = wave;
    const u32x4 xv = *(const u32x4*)(p.x + lane * 8);
    const int rg = j & 3, cg = j >> 2;
    u16 xr[4]; u32x4 wq[4];
#pragma unroll
    for (int i = 0; i < 4; ++i) xr[i] = p.x[rg * 128 + w * 32 + i * 8 + (lane >> 3)];
#pragma unroll
    for (int i = 0; i < 4; ++i) wq[i] = ldnt(p.wq + (size_t)(rg * 128 + w * 32 + i * 8 + (lane >> 3)) * (HQ * DH) + h * 512 + cg * 64 + (lane & 7) * 8);
    const size_t kvoff = (size_t)h * p.kv_head_stride + p.kv_win_off + (size_t)(j * 128 + w * 32) * DH;
    const u16* Kb = p.K + kvoff; const u16* Vb = p.V + kvoff;
    u32x4 kf[8], vf[8];
#pragma unroll
    for (int i = 0; i < 8; ++i) kf[i] = ldnt(Kb + ((i >> 2) * 16 + (lane & 15)) * DH + (i & 3) * 32 + (lane >> 4) * 8);
    SB();
    __builtin_amdgcn_s_barrier();
    SB();
    float m, sd; ln_stats(xv, m, sd);
    STAMP(1);
    float a[8];
#pragma unroll
    for (int e = 0; e < 8; ++e) a[e] = 0.f;
#pragma unroll
    for (int i = 0; i < 4; ++i) {
      const float xn = ln_apply(xr[i], m, sd);
#pragma unroll
      for (int k = 0; k < 4; ++k) { a[2 * k] = fmaf(xn, bf_lo(wq[i][k]), a[2 * k]); a[2 * k + 1] = fmaf(xn, bf_hi(wq[i][k]), a[2 * k + 1]); }
    }
    const float qv = tred8(a, lane);
    sm->qp[w][(lane & 7) * 8 + (lane >> 3)] = qv;
    lds_post(&sm->f_qp, lane);
    STAMP(2);
    SB();
#pragma unroll
    for (int i = 0; i < 8; ++i) vf[i] = ldnt(Vb + ((i >> 2) * 16 + (lane >> 4) * 4 + (i & 3)) * DH + (lane & 15) * 8);
    SB();
    lds_post(&sm->f_visd, lane);
    STAMP(3);
    lds_wait(&sm->f_q, 1);
    STAMP(4);
    u32x4 qf[4];
#pragma unroll
    for (int ii = 0; ii < 4; ++ii) qf[ii] = *(const LAS u32x4*)&sm->qb[(((lane & 15) >> 2) * 128 + ii * 32 + (lane >> 4) * 8) >> 1];
    f32x4 s[2];
#pragma unroll
    for (int t = 0; t < 2; ++t) {
      s[t] = f32x4{0.f, 0.f, 0.f, 0.f};
#pragma unroll
      for (int ii = 0; ii < 4; ++ii)
        s[t] = __builtin_amdgcn_mfma_f32_16x16x32_bf16(__builtin_bit_cast(bf16x8, kf[t * 4 + ii]), __builtin_bit_cast(bf16x8, qf[ii]), s[t], 0, 0, 0);
    }
    float sc[8];
#pragma unroll
    for (int k = 0; k < 8; ++k) sc[k] = rbf(rbf(s[k >> 2][k & 3]) * 0.08838834764831845f) * 1.4426950408889634f;
    float mx = fmaxf(fmaxf(fmaxf(sc[0], sc[1]), fmaxf(sc[2], sc[3])), fmaxf(fmaxf(sc[4], sc[5]), fmaxf(sc[6], sc[7])));
    mx = max32(max16(mx));
    float pr[8], ls = 0.f;
#pragma unroll
    for (int k = 0; k < 8; ++k) { pr[k] = __builtin_amdgcn_exp2f(sc[k] - mx); ls += pr[k]; }
    ls = sum32(sum16(ls));
    if (lane < 16 && (lane & 3) == 0) *(LAS f32x2*)&sm->ml[w][lane >> 2][0] = f32x2{mx, ls};
    const u32x4 pf = {pk_bf16(pr[0], pr[1]), pk_bf16(pr[2], pr[3]), pk_bf16(pr[4], pr[5]), pk_bf16(pr[6], pr[7])};
    STAMP(5);
    float o[8];
#pragma unroll
    for (int e = 0; e < 8; ++e) {
      const u32 sel = (e & 1) ? 0x07060302u : 0x05040100u;
      u32x4 bv;
#pragma unroll
      for (int k = 0; k < 4; ++k) bv[k] = __builtin_amdgcn_perm(vf[2 * k + 1][e >> 1], vf[2 * k][e >> 1], sel);
      const f32x4 d = __builtin_amdgcn_mfma_f32_16x16x32_bf16(__builtin_bit_cast(bf16x8, pf), __builtin_bit_cast(bf16x8, bv), f32x4{0.f, 0.f, 0.f, 0.f}, 0, 0, 0);
      o[e] = d[0];
    }
    {
      LAS float* dst = &sm->op[w][(lane >> 4) * 128 + (lane & 15) * 8];
      *(LAS f32x4*)dst = f32x4{o[0], o[1], o[2], o[3]};
      *(LAS f32x4*)(dst + 4) = f32x4{o[4], o[5], o[6], o[7]};
    }
    lds_post(&sm->f_op, lane);
    STAMP(6);
    lds_wait(&sm->f_op, 4);
    {
      const u32 tag = sm->tag;
      float mw[4], lw[4]; f32x2 ov[4];
#pragma unroll
      for (int k = 0; k < 4; ++k) { const f32x2 t = *(const LAS f32x2*)&sm->ml[k][w][0]; mw[k] = t[0]; lw[k] = t[1]; ov[k] = *(const LAS f32x2*)&sm->op[k][w * 128 + 2 * lane]; }
      const float M = fmaxf(fmaxf(mw[0], mw[1]), fmaxf(mw[2], mw[3]));
      float L = 0.f, o0 = 0.f, o1 = 0.f;
#pragma unroll
      for (int k = 0; k < 4; ++k) { const float wt = __builtin_amdgcn_exp2f(mw[k] - M); L = fmaf(wt, lw[k], L); o0 = fmaf(wt, ov[k][0], o0); o1 = fmaf(wt, ov[k][1], o1); }
      const u32x4 g = {__float_as_uint(o0), tag, __float_as_uint(o1), tag};
      __builtin_amdgcn_raw_buffer_store_b128(g, wsr, WS_OS + (u32)(((h * 32 + w * 8 + (lane >> 3)) * 32 + j) * 128 + (lane & 7) * 16), 0, 16);
      if (lane == 0) {
        const u32x4 gm = {__float_as_uint(M), tag, __float_as_uint(L), tag};
        __builtin_amdgcn_raw_buffer_store_b128(gm, wsr, WS_ML + (u32)(((h * 4 + w) * 32 + j) * 16), 0, 16);
      }
    }
    STAMP(7);
  } else if (wave < 8) {
    const int w = wave - 4;
    const bool ffn = b < 224;
    const int bb = ffn ? b : 0;
    const int lc = bb % 28, rg = bb / 28;
    const u32x4 xv = *(const u32x4*)(p.x + lane * 8);
    u16 xr[2];
#pragma unroll
    for (int i = 0; i < 2; ++i) xr[i] = p.x[rg * 64 + w * 16 + i * 8 + (lane >> 3)];
    SB();
    __builtin_amdgcn_s_barrier();
    SB();
    float m, sd; ln_stats(xv, m, sd);
    float xn[2];
#pragma unroll
    for (int i = 0; i < 2; ++i) xn[i] = ln_apply(xr[i], m, sd);
    STAMP(1);
    lds_wait(&sm->f_visd, 4);
    STAMP(2);
    u32x4 g1[2], g3[2], wo[6];
    if (ffn) {
#pragma unroll
      for (int i = 0; i < 2; ++i) {
        const size_t off = (size_t)(rg * 64 + w * 16 + i * 8 + (lane >> 3)) * FF + lc * 64 + (lane & 7) * 8;
        g1[i] = ldnt(p.w1 + off); g3[i] = ldnt(p.w3 + off);
      }
    }
    const int orow = h * 512 + j * 16;
#pragma unroll
    for (int i = 0; i < 4; ++i) wo[i] = ldnt(p.wo + (size_t)(orow + i * 4 + (lane >> 4)) * DM + w * 128 + (lane & 15) * 8);
#pragma unroll
    for (int i = 0; i < 2; ++i) wo[4 + i] = ldnt(p.w2 + (size_t)(bb * 8 + i * 4 + (lane >> 4)) * DM + w * 128 + (lane & 15) * 8);
    SB();
    STAMP(3);
    if (ffn) {
      float a[8], c[8];
#pragma unroll
      for (int e = 0; e < 8; ++e) { a[e] = 0.f; c[e] = 0.f; }
#pragma unroll
      for (int i = 0; i < 2; ++i)
#pragma unroll
        for (int k = 0; k < 4; ++k) {
          a[2 * k] = fmaf(xn[i], bf_lo(g1[i][k]), a[2 * k]); a[2 * k + 1] = fmaf(xn[i], bf_hi(g1[i][k]), a[2 * k + 1]);
          c[2 * k] = fmaf(xn[i], bf_lo(g3[i][k]), c[2 * k]); c[2 * k + 1] = fmaf(xn[i], bf_hi(g3[i][k]), c[2 * k + 1]);
        }
      const float gv = tred8(a, lane), uv = tred8(c, lane);
      *(LAS f32x2*)&sm->gp[w][((lane & 7) * 8 + (lane >> 3)) * 2] = f32x2{gv, uv};
      lds_post(&sm->f_gp, lane);
      if (w == 0) {
        lds_wait(&sm->f_gp, 4);
        lds_wait(&sm->f_tag, 1);
        const u32 tag = sm->tag;
        f32x2 t = *(const LAS f32x2*)&sm->gp[0][lane * 2];
#pragma unroll
        for (int k = 1; k < 4; ++k) t += *(const LAS f32x2*)&sm->gp[k][lane * 2];
        const u32x4 g = {__float_as_uint(t[0]), tag, __float_as_uint(t[1]), tag};
        __builtin_amdgcn_raw_buffer_store_b128(g, wsr, WS_GS + (u32)((((lc * 8 + (lane >> 3)) * 8 + rg) * 8 + (lane & 7)) * 16), 0, 16);
      }
    }
    STAMP(4);
    float wf[6][8];
#pragma unroll
    for (int i = 0; i < 6; ++i)
#pragma unroll
      for (int k = 0; k < 4; ++k) { wf[i][2 * k] = bf_lo(wo[i][k]); wf[i][2 * k + 1] = bf_hi(wo[i][k]); }
#pragma unroll
    for (int i = 0; i < 6; ++i)
#pragma unroll
      for (int k = 0; k < 8; ++k) asm volatile("" : "+v"(wf[i][k]));
    STAMP(5);
    lds_wait(&sm->f_coef, 1);
    STAMP(6);
    const u32 tag = sm->tag;
    float a[8];
#pragma unroll
    for (int e = 0; e < 8; ++e) a[e] = 0.f;
#pragma unroll
    for (int i = 0; i < 6; ++i) {
      const float cf = sm->coef[i * 4 + (lane >> 4)];
#pragma unroll
      for (int e = 0; e < 8; ++e) a[e] = fmaf(cf, wf[i][e], a[e]);
    }
    const float b0 = tr32(a[0], a[4]), b1 = tr32(a[1], a[5]), b2 = tr32(a[2], a[6]), b3 = tr32(a[3], a[7]);
    const float c0 = tr16(b0, b2), c1 = tr16(b1, b3);
    const u32x4 g = {__float_as_uint(c0), tag, __float_as_uint(c1), tag};
    __builtin_amdgcn_raw_buffer_store_b128(g, wsr, WS_PS + (u32)((b * 256 + w * 64 + (lane & 15) * 4 + (lane >> 5) * 2 + ((lane >> 4) & 1)) * 16), 0, 16);
    STAMP(7);
  } else {
    const u32 ep = __builtin_amdgcn_raw_buffer_load_b32(wsr, WS_EPOCH, 0, 16);
    const int cp = (b & 7) * 32 + (b >> 3);
    const u32 xo = *(const u32*)(p.x + 2 * cp);
    if (lane < 7) (&sm->f_qp)[lane] = 0u;
    asm volatile("s_waitcnt lgkmcnt(0)" ::: "memory");
    SB();
    __builtin_amdgcn_s_barrier();
    SB();
    const u32 tag = ep * 1664525u + 1013904223u;
    if (lane == 0) sm->tag = tag;
    lds_post(&sm->f_tag, lane);
    STAMP(1);
    lds_wait(&sm->f_qp, 4);
    {
      const int rg = j & 3, cg = j >> 2;
      if (lane < 32) {
        f32x2 t = *(const LAS f32x2*)&sm->qp[0][lane * 2];
#pragma unroll
        for (int k = 1; k < 4; ++k) t += *(const LAS f32x2*)&sm->qp[k][lane * 2];
        const u32x4 g = {__float_as_uint(t[0]), tag, __float_as_uint(t[1]), tag};
        __builtin_amdgcn_raw_buffer_store_b128(g, wsr, WS_QS + (u32)(((h * 4 + rg) * 256 + cg * 32 + lane) * 16), 0, 16);
      }
    }
    STAMP(2);
    {
      u32x4 g[16];
#pragma unroll 1
      for (u32 s = 0; s < SPIN_CAP; ++s) {
        CBAR();
        bool ok = true;
#pragma unroll
        for (int k = 0; k < 16; ++k) g[k] = __builtin_amdgcn_raw_buffer_load_b128(wsr, WS_QS + (u32)((h * 1024 + k * 64 + lane) * 16), 0, 16);
#pragma unroll
        for (int k = 0; k < 16; ++k) ok &= (g[k][1] == tag) & (g[k][3] == tag);
        if (__all(ok)) break;
        __builtin_amdgcn_s_sleep(2);
      }
#pragma unroll
      for (int i = 0; i < 4; ++i) {
        const float q0 = (__uint_as_float(g[i][0]) + __uint_as_float(g[4 + i][0])) + (__uint_as_float(g[8 + i][0]) + __uint_as_float(g[12 + i][0]));
        const float q1 = (__uint_as_float(g[i][2]) + __uint_as_float(g[4 + i][2])) + (__uint_as_float(g[8 + i][2]) + __uint_as_float(g[12 + i][2]));
        sm->qb[i * 64 + lane] = pk_bf16(q0, q1);
      }
      lds_post(&sm->f_q, lane);
    }
    STAMP(3);
    lds_wait(&sm->f_op, 4);
    STAMP(4);
    {
      const bool ffn = b < 224;
      u32x4 go[4], gm[4], gg;
#pragma unroll 1
      for (u32 s = 0; s < SPIN_CAP; ++s) {
        CBAR();
        bool ok = true;
#pragma unroll
        for (int k = 0; k < 4; ++k) go[k] = __builtin_amdgcn_raw_buffer_load_b128(wsr, WS_OS + (u32)(((h * 32 + j) * 32) * 128 + (k * 64 + lane) * 16), 0, 16);
#pragma unroll
        for (int k = 0; k < 4; ++k) gm[k] = __builtin_amdgcn_raw_buffer_load_b128(wsr, WS_ML + (u32)(((h * 4 + (j >> 3)) * 32 + k * 8 + (lane >> 3)) * 16), 0, 16);
        gg = __builtin_amdgcn_raw_buffer_load_b128(wsr, WS_GS + (u32)(((ffn ? b : 0) * 64 + lane) * 16), 0, 16);
#pragma unroll
        for (int k = 0; k < 4; ++k) ok &= (go[k][1] == tag) & (go[k][3] == tag) & (gm[k][1] == tag) & (gm[k][3] == tag);
        ok &= (gg[1] == tag) & (gg[3] == tag);
        if (__all(ok)) break;
        __builtin_amdgcn_s_sleep(1);
      }
      STAMP(5);
      float M = fmaxf(fmaxf(__uint_as_float(gm[0][0]), __uint_as_float(gm[1][0])), fmaxf(__uint_as_float(gm[2][0]), __uint_as_float(gm[3][0])));
      M = fmaxf(M, dpp<XOR8>(M)); M = max32(max16(M));
      float L = 0.f, o0 = 0.f, o1 = 0.f;
#pragma unroll
      for (int k = 0; k < 4; ++k) {
        const float wt = __builtin_amdgcn_exp2f(__uint_as_float(gm[k][0]) - M);
        L = fmaf(wt, __uint_as_float(gm[k][2]), L); o0 = fmaf(wt, __uint_as_float(go[k][0]), o0); o1 = fmaf(wt, __uint_as_float(go[k][2]), o1);
      }
      float gt = __uint_as_float(gg[0]), up = __uint_as_float(gg[2]);
      L += dpp<XOR8>(L); o0 += dpp<XOR8>(o0); o1 += dpp<XOR8>(o1); gt += dpp<XOR8>(gt); up += dpp<XOR8>(up);
      L = sum32(sum16(L)); o0 = sum32(sum16(o0)); o1 = sum32(sum16(o1)); gt = sum32(sum16(gt)); up = sum32(sum16(up));
      const float inv = 1.f / L;
      gt = rbf(gt); up = rbf(up);
      const float sl = rbf(gt / (1.f + __expf(-gt)));
      const float hv = ffn ? rbf(sl * up) : 0.f;
      if (lane < 8) { *(LAS f32x2*)&sm->coef[lane * 2] = f32x2{rbf(o0 * inv), rbf(o1 * inv)}; sm->coef[16 + lane] = hv; }
      lds_post(&sm->f_coef, lane);
    }
    STAMP(6);
    {
      u32x4 g[4];
#pragma unroll 1
      for (u32 s = 0; s < SPIN_CAP; ++s) {
        CBAR();
        bool ok = true;
#pragma unroll
        for (int k = 0; k < 4; ++k) g[k] = __builtin_amdgcn_raw_buffer_load_b128(wsr, WS_PS + (u32)(((k * 64 + lane) * 256 + cp) * 16), 0, 16);
#pragma unroll
        for (int k = 0; k < 4; ++k) ok &= (g[k][1] == tag) & (g[k][3] == tag);
        if (__all(ok)) break;
        __builtin_amdgcn_s_sleep(1);
      }
      STAMP(7);
      float s0 = (__uint_as_float(g[0][0]) + __uint_as_float(g[1][0])) + (__uint_as_float(g[2][0]) + __uint_as_float(g[3][0]));
      float s1 = (__uint_as_float(g[0][2]) + __uint_as_float(g[1][2])) + (__uint_as_float(g[2][2]) + __uint_as_float(g[3][2]));
      s0 = wave_sum(s0); s1 = wave_sum(s1);
      if (lane == 0) {
        *(u32*)(p.out + 2 * cp) = pk_bf16(bf_lo(xo) + s0, bf_hi(xo) + s1);
        if (b == 0) __builtin_amdgcn_raw_buffer_store_b32(tag, wsr, WS_EPOCH, 0, 16);
      }
      STAMP(8);
    }
  }
  STAMP_FLUSH();
}
}

extern "C" void kernel_launch(void* const* d_in, const int* in_sizes, int n_in, void* d_out, int out_size, void* d_ws,
                              size_t ws_size, hipStream_t stream) {
  Params p;
  p.x = (const u16*)d_in[0]; p.K = (const u16*)d_in[1]; p.V = (const u16*)d_in[2];
  p.wq = (const u16*)d_in[3]; p.wo = (const u16*)d_in[4];
  p.w1 = (const u16*)d_in[6]; p.w3 = (const u16*)d_in[7]; p.w2 = (const u16*)d_in[8];
  p.out = (u16*)d_out; p.ws = (unsigned char*)d_ws;
  const u64 sk = (u64)in_sizes[1] / (HKV * DH);
  p.kv_head_stride = sk * DH;
  p.kv_win_off = (sk - WIN) * DH;
  hipLaunchKernelGGL(fused_decode, dim3(NWG), dim3(NTHR), 0, stream, p);
}
```

```cpp
#define DIAG 1
#define PROBE 0
#define PROBE_B 0
#define PROBE_W 8
#include <hip/hip_runtime.h>
#include <hip/hip_bf16.h>
#include <stdint.h>

typedef unsigned short u16;
typedef unsigned u32;
typedef unsigned long long u64;
typedef u32 u32x4 __attribute__((ext_vector_type(4)));
typedef u32 u32x2 __attribute__((ext_vector_type(2)));
typedef float f32x4 __attribute__((ext_vector_type(4)));
typedef float f32x2 __attribute__((ext_vector_type(2)));
typedef __bf16 bf16x8 __attribute__((ext_vector_type(8)));
#define LAS __attribute__((address_space(3)))
#define GAS __attribute__((address_space(1)))

namespace {
constexpr int DM = 512, HQ = 32, HKV = 8, DH = 128, WIN = 4096, FF = 1792;
constexpr int NWG = 256, NTHR = 576;
constexpr u32 WS_EPOCH = 0;
constexpr u32 WS_QS = 4096;
constexpr u32 WS_ML = WS_QS + 8 * 4 * 512 * 8;
constexpr u32 WS_GS = WS_ML + 8 * 4 * 32 * 16;
constexpr u32 WS_OS = WS_GS + 224 * 8 * 8 * 16;
constexpr u32 WS_PS = WS_OS + 8 * 32 * 32 * 128;
constexpr u32 WS_END = WS_PS + 256 * 256 * 16;
constexpr u32 WS_DIAG = 8u << 20;
constexpr u32 SPIN_CAP = 1u << 22;

struct Params {
  const u16 *x, *K, *V, *wq, *wo, *w1, *w3, *w2;
  u16* out;
  unsigned char* ws;
  u64 kv_head_stride;
  u64 kv_win_off;
};

__device__ __forceinline__ float bf_lo(u32 v) { return __uint_as_float(v << 16); }
__device__ __forceinline__ float bf_hi(u32 v) { return __uint_as_float(v & 0xffff0000u); }
__device__ __forceinline__ u32 pk_bf16(float lo, float hi) {
  typedef __bf16 bf16x2 __attribute__((ext_vector_type(2)));
  bf16x2 r = {(__bf16)lo, (__bf16)hi};
  return __builtin_bit_cast(u32, r);
}
__device__ __forceinline__ float rbf(float f) { return (float)(__bf16)f; }
template <int CTRL> __device__ __forceinline__ float dpp(float x) {
  return __builtin_bit_cast(float, __builtin_amdgcn_mov_dpp(__builtin_bit_cast(int, x), CTRL, 0xf, 0xf, true));
}
constexpr int XOR1 = 0xB1, XOR2 = 0x4E, XOR7 = 0x141, XOR8 = 0x128, ROR4 = 0x124;
__device__ __forceinline__ float tr32(float a, float b) {
  auto s = __builtin_amdgcn_permlane32_swap(__float_as_uint(a), __float_as_uint(b), false, false);
  return __uint_as_float(s[0]) + __uint_as_float(s[1]);
}
__device__ __forceinline__ float tr16(float a, float b) {
  auto s = __builtin_amdgcn_permlane16_swap(__float_as_uint(a), __float_as_uint(b), false, false);
  return __uint_as_float(s[0]) + __uint_as_float(s[1]);
}
__device__ __forceinline__ float sum32(float x) { return tr32(x, x); }
__device__ __forceinline__ float sum16(float x) { return tr16(x, x); }
__device__ __forceinline__ float max32(float x) {
  auto s = __builtin_amdgcn_permlane32_swap(__float_as_uint(x), __float_as_uint(x), false, false);
  return fmaxf(__uint_as_float(s[0]), __uint_as_float(s[1]));
}
__device__ __forceinline__ float max16(float x) {
  auto s = __builtin_amdgcn_permlane16_swap(__float_as_uint(x), __float_as_uint(x), false, false);
  return fmaxf(__uint_as_float(s[0]), __uint_as_float(s[1]));
}
__device__ __forceinline__ float wave_sum(float x) {
  x += dpp<XOR1>(x); x += dpp<XOR2>(x); x += dpp<XOR7>(x); x += dpp<XOR8>(x);
  return sum32(sum16(x));
}
__device__ __forceinline__ u32x4 ldnt(const void* p) { return __builtin_nontemporal_load((const u32x4*)p); }
#define CBAR() asm volatile("" ::: "memory")
#define SB() __builtin_amdgcn_sched_barrier(0)

__device__ __forceinline__ u64 rt() { SB(); u64 t; asm volatile("s_memrealtime %0\n\ts_waitcnt lgkmcnt(0)" : "=s"(t)::"memory"); SB(); return t; }
#define STAMP(i) do { ts[i] = rt(); } while (0)
#define STAMP_DECL u64 ts[12] = {0,0,0,0,0,0,0,0,0,0,0,0}
#define STAMP_FLUSH() do { if (b == PROBE_B && wave == PROBE_W) { const u64 until = rt() + 10ull * (ts[PROBE] - ts[0]); while (rt() < until) __builtin_amdgcn_s_sleep(4); } } while (0)

struct Lds {
  float qp[4][64];
  float gp[4][128];
  u32 qb[256];
  float op[4][512];
  float ml[4][4][2];
  float coef[24];
  u32 tag;
  u32 f_qp, f_gp, f_q, f_op, f_coef, f_visd, f_tag;
};

__device__ __forceinline__ void lds_wait(volatile LAS u32* f, u32 want) {
#pragma unroll 1
  for (u32 s = 0; s < SPIN_CAP; ++s) {
    if (*f >= want) break;
    __builtin_amdgcn_s_sleep(1);
  }
  CBAR();
}
__device__ __forceinline__ void lds_post(LAS u32* f, int lane) {
  CBAR();
  if (lane == 0) __hip_atomic_fetch_add(f, 1u, __ATOMIC_RELAXED, __HIP_MEMORY_SCOPE_WORKGROUP);
  CBAR();
}

__device__ __forceinline__ void ln_stats(u32x4 xv, float& m, float& sd) {
  float e[8];
#pragma unroll
  for (int k = 0; k < 4; ++k) { e[2 * k] = bf_lo(xv[k]); e[2 * k + 1] = bf_hi(xv[k]); }
  float s = ((e[0] + e[1]) + (e[2] + e[3])) + ((e[4] + e[5]) + (e[6] + e[7]));
  const float mean = wave_sum(s) * (1.f / DM);
  float v = 0.f;
#pragma unroll
  for (int k = 0; k < 8; ++k) { const float d = e[k] - mean; v = fmaf(d, d, v); }
  const float var = wave_sum(v) * (1.f / DM);
  m = rbf(mean);
  sd = rbf(sqrtf(rbf(rbf(var) + 1e-5f)));
}
__device__ __forceinline__ float ln_apply(u16 xr, float m, float sd) {
  return rbf(rbf(__uint_as_float((u32)xr << 16) - m) / sd);
}
__device__ __forceinline__ float tred8(const float (&a)[8], int lane) {
  const float b0 = tr32(a[0], a[4]), b1 = tr32(a[1], a[5]), b2 = tr32(a[2], a[6]), b3 = tr32(a[3], a[7]);
  const float c0 = tr16(b0, b2), c1 = tr16(b1, b3);
  const float d0 = c0 + dpp<XOR8>(c0), d1 = c1 + dpp<XOR8>(c1);
  return (lane & 8) ? d1 : d0;
}

__global__ void __launch_bounds__(NTHR) fused_decode(Params p) {
  __shared__ __attribute__((aligned(16))) Lds sm_;
  LAS Lds* sm = (LAS Lds*)&sm_;
  const int b = blockIdx.x, tid = threadIdx.x, lane = tid & 63;
  const int wave = __builtin_amdgcn_readfirstlane(tid >> 6);
  const int h = b & 7, j = b >> 3;
  const __amdgpu_buffer_rsrc_t wsr = __builtin_amdgcn_make_buffer_rsrc(p.ws, 0, WS_END, 0x00020000);
  STAMP_DECL;
  STAMP(0);

  if (wave < 4) {
    const int w = wave;
    const u32x4 xv = *(const u32x4*)(p.x + lane * 8);
    const int rg = j & 3, cg = j >> 2;
    u16 xr[4]; u32x4 wq[4];
#pragma unroll
    for (int i = 0; i < 4; ++i) xr[i] = p.x[rg * 128 + w * 32 + i * 8 + (lane >> 3)];
#pragma unroll
    for (int i = 0; i < 4; ++i) wq[i] = ldnt(p.wq + (size_t)(rg * 128 + w * 32 + i * 8 + (lane >> 3)) * (HQ * DH) + h * 512 + cg * 64 + (lane & 7) * 8);
    const size_t kvoff = (size_t)h * p.kv_head_stride + p.kv_win_off + (size_t)(j * 128 + w * 32) * DH;
    const u16* Kb = p.K + kvoff; const u16* Vb = p.V + kvoff;
    u32x4 kf[8], vf[8];
#pragma unroll
    for (int i = 0; i < 8; ++i) kf[i] = ldnt(Kb + ((i >> 2) * 16 + (lane & 15)) * DH + (i & 3) * 32 + (lane >> 4) * 8);
    SB();
    __builtin_amdgcn_s_barrier();
    SB();
    float m, sd; ln_stats(xv, m, sd);
    STAMP(1);
    float a[8];
#pragma unroll
    for (int e = 0; e < 8; ++e) a[e] = 0.f;
#pragma unroll
    for (int i = 0; i < 4; ++i) {
      const float xn = ln_apply(xr[i], m, sd);
#pragma unroll
      for (int k = 0; k < 4; ++k) { a[2 * k] = fmaf(xn, bf_lo(wq[i][k]), a[2 * k]); a[2 * k + 1] = fmaf(xn, bf_hi(wq[i][k]), a[2 * k + 1]); }
    }
    const float qv = tred8(a, lane);
    sm->qp[w][(lane & 7) * 8 + (lane >> 3)] = qv;
    lds_post(&sm->f_qp, lane);
    STAMP(2);
    SB();
#pragma unroll
    for (int i = 0; i < 8; ++i) vf[i] = ldnt(Vb + ((i >> 2) * 16 + (lane >> 4) * 4 + (i & 3)) * DH + (lane & 15) * 8);
    SB();
    lds_post(&sm->f_visd, lane);
    STAMP(3);
    lds_wait(&sm->f_q, 1);
    STAMP(4);
    u32x4 qf[4];
#pragma unroll
    for (int ii = 0; ii < 4; ++ii) qf[ii] = *(const LAS u32x4*)&sm->qb[(((lane & 15) >> 2) * 128 + ii * 32 + (lane >> 4) * 8) >> 1];
    f32x4 s[2];
#pragma unroll
    for (int t = 0; t < 2; ++t) {
      s[t] = f32x4{0.f, 0.f, 0.f, 0.f};
#pragma unroll
      for (int ii = 0; ii < 4; ++ii)
        s[t] = __builtin_amdgcn_mfma_f32_16x16x32_bf16(__builtin_bit_cast(bf16x8, kf[t * 4 + ii]), __builtin_bit_cast(bf16x8, qf[ii]), s[t], 0, 0, 0);
    }
    float sc[8];
#pragma unroll
    for (int k = 0; k < 8; ++k) sc[k] = rbf(rbf(s[k >> 2][k & 3]) * 0.08838834764831845f) * 1.4426950408889634f;
    float mx = fmaxf(fmaxf(fmaxf(sc[0], sc[1]), fmaxf(sc[2], sc[3])), fmaxf(fmaxf(sc[4], sc[5]), fmaxf(sc[6], sc[7])));
    mx = max32(max16(mx));
    float pr[8], ls = 0.f;
#pragma unroll
    for (int k = 0; k < 8; ++k) { pr[k] = __builtin_amdgcn_exp2f(sc[k] - mx); ls += pr[k]; }
    ls = sum32(sum16(ls));
    if (lane < 16 && (lane & 3) == 0) *(LAS f32x2*)&sm->ml[w][lane >> 2][0] = f32x2{mx, ls};
    const u32x4 pf = {pk_bf16(pr[0], pr[1]), pk_bf16(pr[2], pr[3]), pk_bf16(pr[4], pr[5]), pk_bf16(pr[6], pr[7])};
    STAMP(5);
    float o[8];
#pragma unroll
    for (int e = 0; e < 8; ++e) {
      const u32 sel = (e & 1) ? 0x07060302u : 0x05040100u;
      u32x4 bv;
#pragma unroll
      for (int k = 0; k < 4; ++k) bv[k] = __builtin_amdgcn_perm(vf[2 * k + 1][e >> 1], vf[2 * k][e >> 1], sel);
      const f32x4 d = __builtin_amdgcn_mfma_f32_16x16x32_bf16(__builtin_bit_cast(bf16x8, pf), __builtin_bit_cast(bf16x8, bv), f32x4{0.f, 0.f, 0.f, 0.f}, 0, 0, 0);
      o[e] = d[0];
    }
    {
      LAS float* dst = &sm->op[w][(lane >> 4) * 128 + (lane & 15) * 8];
      *(LAS f32x4*)dst = f32x4{o[0], o[1], o[2], o[3]};
      *(LAS f32x4*)(dst + 4) = f32x4{o[4], o[5], o[6], o[7]};
    }
    lds_post(&sm->f_op, lane);
    STAMP(6);
    lds_wait(&sm->f_op, 4);
    {
      const u32 tag = sm->tag;
      float mw[4], lw[4]; f32x2 ov[4];
#pragma unroll
      for (int k = 0; k < 4; ++k) { const f32x2 t = *(const LAS f32x2*)&sm->ml[k][w][0]; mw[k] = t[0]; lw[k] = t[1]; ov[k] = *(const LAS f32x2*)&sm->op[k][w * 128 + 2 * lane]; }
      const float M = fmaxf(fmaxf(mw[0], mw[1]), fmaxf(mw[2], mw[3]));
      float L = 0.f, o0 = 0.f, o1 = 0.f;
#pragma unroll
      for (int k = 0; k < 4; ++k) { const float wt = __builtin_amdgcn_exp2f(mw[k] - M); L = fmaf(wt, lw[k], L); o0 = fmaf(wt, ov[k][0], o0); o1 = fmaf(wt, ov[k][1], o1); }
      const u32x4 g = {__float_as_uint(o0), tag, __float_as_uint(o1), tag};
      __builtin_amdgcn_raw_buffer_store_b128(g, wsr, WS_OS + (u32)(((h * 32 + w * 8 + (lane >> 3)) * 32 + j) * 128 + (lane & 7) * 16), 0, 16);
      if (lane == 0) {
        const u32x4 gm = {__float_as_uint(M), tag, __float_as_uint(L), tag};
        __builtin_amdgcn_raw_buffer_store_b128(gm, wsr, WS_ML + (u32)(((h * 4 + w) * 32 + j) * 16), 0, 16);
      }
    }
    STAMP(7);
  } else if (wave < 8) {
    const int w = wave - 4;
    const bool ffn = b < 224;
    const int bb = ffn ? b : 0;
    const int lc = bb % 28, rg = bb / 28;
    const u32x4 xv = *(const u32x4*)(p.x + lane * 8);
    u16 xr[2];
#pragma unroll
    for (int i = 0; i < 2; ++i) xr[i] = p.x[rg * 64 + w * 16 + i * 8 + (lane >> 3)];
    SB();
    __builtin_amdgcn_s_barrier();
    SB();
    float m, sd; ln_stats(xv, m, sd);
    float xn[2];
#pragma unroll
    for (int i = 0; i < 2; ++i) xn[i] = ln_apply(xr[i], m, sd);
    STAMP(1);
    lds_wait(&sm->f_visd, 4);
    STAMP(2);
    u32x4 g1[2], g3[2], wo[6];
    if (ffn) {
#pragma unroll
      for (int i = 0; i < 2; ++i) {
        const size_t off = (size_t)(rg * 64 + w * 16 + i * 8 + (lane >> 3)) * FF + lc * 64 + (lane & 7) * 8;
        g1[i] = ldnt(p.w1 + off); g3[i] = ldnt(p.w3 + off);
      }
    }
    const int orow = h * 512 + j * 16;
#pragma unroll
    for (int i = 0; i < 4; ++i) wo[i] = ldnt(p.wo + (size_t)(orow + i * 4 + (lane >> 4)) * DM + w * 128 + (lane & 15) * 8);
#pragma unroll
    for (int i = 0; i < 2; ++i) wo[4 + i] = ldnt(p.w2 + (size_t)(bb * 8 + i * 4 + (lane >> 4)) * DM + w * 128 + (lane & 15) * 8);
    SB();
    STAMP(3);
    if (ffn) {
      float a[8], c[8];
#pragma unroll
      for (int e = 0; e < 8; ++e) { a[e] = 0.f; c[e] = 0.f; }
#pragma unroll
      for (int i = 0; i < 2; ++i)
#pragma unroll
        for (int k = 0; k < 4; ++k) {
          a[2 * k] = fmaf(xn[i], bf_lo(g1[i][k]), a[2 * k]); a[2 * k + 1] = fmaf(xn[i], bf_hi(g1[i][k]), a[2 * k + 1]);
          c[2 * k] = fmaf(xn[i], bf_lo(g3[i][k]), c[2 * k]); c[2 * k + 1] = fmaf(xn[i], bf_hi(g3[i][k]), c[2 * k + 1]);
        }
      const float gv = tred8(a, lane), uv = tred8(c, lane);
      *(LAS f32x2*)&sm->gp[w][((lane & 7) * 8 + (lane >> 3)) * 2] = f32x2{gv, uv};
      lds_post(&sm->f_gp, lane);
      if (w == 0) {
        lds_wait(&sm->f_gp, 4);
        lds_wait(&sm->f_tag, 1);
        const u32 tag = sm->tag;
        f32x2 t = *(const LAS f32x2*)&sm->gp[0][lane * 2];
#pragma unroll
        for (int k = 1; k < 4; ++k) t += *(const LAS f32x2*)&sm->gp[k][lane * 2];
        const u32x4 g = {__float_as_uint(t[0]), tag, __float_as_uint(t[1]), tag};
        __builtin_amdgcn_raw_buffer_store_b128(g, wsr, WS_GS + (u32)((((lc * 8 + (lane >> 3)) * 8 + rg) * 8 + (lane & 7)) * 16), 0, 16);
      }
    }
    STAMP(4);
    float wf[6][8];
#pragma unroll
    for (int i = 0; i < 6; ++i)
#pragma unroll
      for (int k = 0; k < 4; ++k) { wf[i][2 * k] = bf_lo(wo[i][k]); wf[i][2 * k + 1] = bf_hi(wo[i][k]); }
#pragma unroll
    for (int i = 0; i < 6; ++i)
#pragma unroll
      for (int k = 0; k < 8; ++k) asm volatile("" : "+v"(wf[i][k]));
    STAMP(5);
    lds_wait(&sm->f_coef, 1);
    STAMP(6);
    const u32 tag = sm->tag;
    float a[8];
#pragma unroll
    for (int e = 0; e < 8; ++e) a[e] = 0.f;
#pragma unroll
    for (int i = 0; i < 6; ++i) {
      const float cf = sm->coef[i * 4 + (lane >> 4)];
#pragma unroll
      for (int e = 0; e < 8; ++e) a[e] = fmaf(cf, wf[i][e], a[e]);
    }
    const float b0 = tr32(a[0], a[4]), b1 = tr32(a[1], a[5]), b2 = tr32(a[2], a[6]), b3 = tr32(a[3], a[7]);
    const float c0 = tr16(b0, b2), c1 = tr16(b1, b3);
    const u32x4 g = {__float_as_uint(c0), tag, __float_as_uint(c1), tag};
    __builtin_amdgcn_raw_buffer_store_b128(g, wsr, WS_PS + (u32)((b * 256 + w * 64 + (lane & 15) * 4 + (lane >> 5) * 2 + ((lane >> 4) & 1)) * 16), 0, 16);
    STAMP(7);
  } else {
    const u32 ep = __builtin_amdgcn_raw_buffer_load_b32(wsr, WS_EPOCH, 0, 16);
    const int cp = (b & 7) * 32 + (b >> 3);
    const u32 xo = *(const u32*)(p.x + 2 * cp);
    if (lane < 7) (&sm->f_qp)[lane] = 0u;
    asm volatile("s_waitcnt lgkmcnt(0)" ::: "memory");
    SB();
    __builtin_amdgcn_s_barrier();
    SB();
    const u32 tag = ep * 1664525u + 1013904223u;
    if (lane == 0) sm->tag = tag;
    lds_post(&sm->f_tag, lane);
    STAMP(1);
    lds_wait(&sm->f_qp, 4);
    {
      const int rg = j & 3, cg = j >> 2;
      if (lane < 32) {
        f32x2 t = *(const LAS f32x2*)&sm->qp[0][lane * 2];
#pragma unroll
        for (int k = 1; k < 4; ++k) t += *(const LAS f32x2*)&sm->qp[k][lane * 2];
        const u32x4 g = {__float_as_uint(t[0]), tag, __float_as_uint(t[1]), tag};
        __builtin_amdgcn_raw_buffer_store_b128(g, wsr, WS_QS + (u32)(((h * 4 + rg) * 256 + cg * 32 + lane) * 16), 0, 16);
      }
    }
    STAMP(2);
    {
      u32x4 g[16];
#pragma unroll 1
      for (u32 s = 0; s < SPIN_CAP; ++s) {
        CBAR();
        bool ok = true;
#pragma unroll
        for (int k = 0; k < 16; ++k) g[k] = __builtin_amdgcn_raw_buffer_load_b128(wsr, WS_QS + (u32)((h * 1024 + k * 64 + lane) * 16), 0, 16);
#pragma unroll
        for (int k = 0; k < 16; ++k) ok &= (g[k][1] == tag) & (g[k][3] == tag);
        if (__all(ok)) break;
        __builtin_amdgcn_s_sleep(2);
      }
#pragma unroll
      for (int i = 0; i < 4; ++i) {
        const float q0 = (__uint_as_float(g[i][0]) + __uint_as_float(g[4 + i][0])) + (__uint_as_float(g[8 + i][0]) + __uint_as_float(g[12 + i][0]));
        const float q1 = (__uint_as_float(g[i][2]) + __uint_as_float(g[4 + i][2])) + (__uint_as_float(g[8 + i][2]) + __uint_as_float(g[12 + i][2]));
        sm->qb[i * 64 + lane] = pk_bf16(q0, q1);
      }
      lds_post(&sm->f_q, lane);
    }
    STAMP(3);
    lds_wait(&sm->f_op, 4);
    STAMP(4);
    {
      const bool ffn = b < 224;
      u32x4 go[4], gm[4], gg;
#pragma unroll 1
      for (u32 s = 0; s < SPIN_CAP; ++s) {
        CBAR();
        bool ok = true;
#pragma unroll
        for (int k = 0; k < 4; ++k) go[k] = __builtin_amdgcn_raw_buffer_load_b128(wsr, WS_OS + (u32)(((h * 32 + j) * 32) * 128 + (k * 64 + lane) * 16), 0, 16);
#pragma unroll
        for (int k = 0; k < 4; ++k) gm[k] = __builtin_amdgcn_raw_buffer_load_b128(wsr, WS_ML + (u32)(((h * 4 + (j >> 3)) * 32 + k * 8 + (lane >> 3)) * 16), 0, 16);
        gg = __builtin_amdgcn_raw_buffer_load_b128(wsr, WS_GS + (u32)(((ffn ? b : 0) * 64 + lane) * 16), 0, 16);
#pragma unroll
        for (int k = 0; k < 4; ++k) ok &= (go[k][1] == tag) & (go[k][3] == tag) & (gm[k][1] == tag) & (gm[k][3] == tag);
        ok &= (gg[1] == tag) & (gg[3] == tag);
        if (__all(ok)) break;
        __builtin_amdgcn_s_sleep(1);
      }
      STAMP(5);
      float M = fmaxf(fmaxf(__uint_as_float(gm[0][0]), __uint_as_float(gm[1][0])), fmaxf(__uint_as_float(gm[2][0]), __uint_as_float(gm[3][0])));
      M = fmaxf(M, dpp<XOR8>(M)); M = max32(max16(M));
      float L = 0.f, o0 = 0.f, o1 = 0.f;
#pragma unroll
      for (int k = 0; k < 4; ++k) {
        const float wt = __builtin_amdgcn_exp2f(__uint_as_float(gm[k][0]) - M);
        L = fmaf(wt, __uint_as_float(gm[k][2]), L); o0 = fmaf(wt, __uint_as_float(go[k][0]), o0); o1 = fmaf(wt, __uint_as_float(go[k][2]), o1);
      }
      float gt = __uint_as_float(gg[0]), up = __uint_as_float(gg[2]);
      L += dpp<XOR8>(L); o0 += dpp<XOR8>(o0); o1 += dpp<XOR8>(o1); gt += dpp<XOR8>(gt); up += dpp<XOR8>(up);
      L = sum32(sum16(L)); o0 = sum32(sum16(o0)); o1 = sum32(sum16(o1)); gt = sum32(sum16(gt)); up = sum32(sum16(up));
      const float inv = 1.f / L;
      gt = rbf(gt); up = rbf(up);
      const float sl = rbf(gt / (1.f + __expf(-gt)));
      const float hv = ffn ? rbf(sl * up) : 0.f;
      if (lane < 8) { *(LAS f32x2*)&sm->coef[lane * 2] = f32x2{rbf(o0 * inv), rbf(o1 * inv)}; sm->coef[16 + lane] = hv; }
      lds_post(&sm->f_coef, lane);
    }
    STAMP(6);
    {
      u32x4 g[4];
#pragma unroll 1
      for (u32 s = 0; s < SPIN_CAP; ++s) {
        CBAR();
        bool ok = true;
#pragma unroll
        for (int k = 0; k < 4; ++k) g[k] = __builtin_amdgcn_raw_buffer_load_b128(wsr, WS_PS + (u32)(((k * 64 + lane) * 256 + cp) * 16), 0, 16);
#pragma unroll
        for (int k = 0; k < 4; ++k) ok &= (g[k][1] == tag) & (g[k][3] == tag);
        if (__all(ok)) break;
        __builtin_amdgcn_s_sleep(1);
      }
      STAMP(7);
      float s0 = (__uint_as_float(g[0][0]) + __uint_as_float(g[1][0])) + (__uint_as_float(g[2][0]) + __uint_as_float(g[3][0]));
      float s1 = (__uint_as_float(g[0][2]) + __uint_as_float(g[1][2])) + (__uint_as_float(g[2][2]) + __uint_as_float(g[3][2]));
      s0 = wave_sum(s0); s1 = wave_sum(s1);
      if (lane == 0) {
        *(u32*)(p.out + 2 * cp) = pk_bf16(bf_lo(xo) + s0, bf_hi(xo) + s1);
        if (b == 0) __builtin_amdgcn_raw_buffer_store_b32(tag, wsr, WS_EPOCH, 0, 16);
      }
      STAMP(8);
    }
  }
  STAMP_FLUSH();
}
}

extern "C" void kernel_launch(void* const* d_in, const int* in_sizes, int n_in, void* d_out, int out_size, void* d_ws,
                              size_t ws_size, hipStream_t stream) {
  Params p;
  p.x = (const u16*)d_in[0]; p.K = (const u16*)d_in[1]; p.V = (const u16*)d_in[2];
  p.wq = (const u16*)d_in[3]; p.wo = (const u16*)d_in[4];
  p.w1 = (const u16*)d_in[6]; p.w3 = (const u16*)d_in[7]; p.w2 = (const u16*)d_in[8];
  p.out = (u16*)d_out; p.ws = (unsigned char*)d_ws;
  const u64 sk = (u64)in_sizes[1] / (HKV * DH);
  p.kv_head_stride = sk * DH;
  p.kv_win_off = (sk - WIN) * DH;
  hipLaunchKernelGGL(fused_decode, dim3(NWG), dim3(NTHR), 0, stream, p);
}
```

```cpp
#define DIAG 1
#define PROBE 8
#define PROBE_B 0
#define PROBE_W 8
#include <hip/hip_runtime.h>
#include <hip/hip_bf16.h>
#include <stdint.h>

typedef unsigned short u16;
typedef unsigned u32;
typedef unsigned long long u64;
typedef u32 u32x4 __attribute__((ext_vector_type(4)));
typedef u32 u32x2 __attribute__((ext_vector_type(2)));
typedef float f32x4 __attribute__((ext_vector_type(4)));
typedef float f32x2 __attribute__((ext_vector_type(2)));
typedef __bf16 bf16x8 __attribute__((ext_vector_type(8)));
#define LAS __attribute__((address_space(3)))
#define GAS __attribute__((address_space(1)))

namespace {
constexpr int DM = 512, HQ = 32, HKV = 8, DH = 128, WIN = 4096, FF = 1792;
constexpr int NWG = 256, NTHR = 576;
constexpr u32 WS_EPOCH = 0;
constexpr u32 WS_QS = 4096;
constexpr u32 WS_ML = WS_QS + 8 * 4 * 512 * 8;
constexpr u32 WS_GS = WS_ML + 8 * 4 * 32 * 16;
constexpr u32 WS_OS = WS_GS + 224 * 8 * 8 * 16;
constexpr u32 WS_PS = WS_OS + 8 * 32 * 32 * 128;
constexpr u32 WS_END = WS_PS + 256 * 256 * 16;
constexpr u32 WS_DIAG = 8u << 20;
constexpr u32 SPIN_CAP = 1u << 22;

struct Params {
  const u16 *x, *K, *V, *wq, *wo, *w1, *w3, *w2;
  u16* out;
  unsigned char* ws;
  u64 kv_head_stride;
  u64 kv_win_off;
};

__device__ __forceinline__ float bf_lo(u32 v) { return __uint_as_float(v << 16); }
__device__ __forceinline__ float bf_hi(u32 v) { return __uint_as_float(v & 0xffff0000u); }
__device__ __forceinline__ u32 pk_bf16(float lo, float hi) {
  typedef __bf16 bf16x2 __attribute__((ext_vector_type(2)));
  bf16x2 r = {(__bf16)lo, (__bf16)hi};
  return __builtin_bit_cast(u32, r);
}
__device__ __forceinline__ float rbf(float f) { return (float)(__bf16)f; }
template <int CTRL> __device__ __forceinline__ float dpp(float x) {
  return __builtin_bit_cast(float, __builtin_amdgcn_mov_dpp(__builtin_bit_cast(int, x), CTRL, 0xf, 0xf, true));
}
constexpr int XOR1 = 0xB1, XOR2 = 0x4E, XOR7 = 0x141, XOR8 = 0x128, ROR4 = 0x124;
__device__ __forceinline__ float tr32(float a, float b) {
  auto s = __builtin_amdgcn_permlane32_swap(__float_as_uint(a), __float_as_uint(b), false, false);
  return __uint_as_float(s[0]) + __uint_as_float(s[1]);
}
__device__ __forceinline__ float tr16(float a, float b) {
  auto s = __builtin_amdgcn_permlane16_swap(__float_as_uint(a), __float_as_uint(b), false, false);
  return __uint_as_float(s[0]) + __uint_as_float(s[1]);
}
__device__ __forceinline__ float sum32(float x) { return tr32(x, x); }
__device__ __forceinline__ float sum16(float x) { return tr16(x, x); }
__device__ __forceinline__ float max32(float x) {
  auto s = __builtin_amdgcn_permlane32_swap(__float_as_uint(x), __float_as_uint(x), false, false);
  return fmaxf(__uint_as_float(s[0]), __uint_as_float(s[1]));
}
__device__ __forceinline__ float max16(float x) {
  auto s = __builtin_amdgcn_permlane16_swap(__float_as_uint(x), __float_as_uint(x), false, false);
  return fmaxf(__uint_as_float(s[0]), __uint_as_float(s[1]));
}
__device__ __forceinline__ float wave_sum(float x) {
  x += dpp<XOR1>(x); x += dpp<XOR2>(x); x += dpp<XOR7>(x); x += dpp<XOR8>(x);
  return sum32(sum16(x));
}
__device__ __forceinline__ u32x4 ldnt(const void* p) { return __builtin_nontemporal_load((const u32x4*)p); }
#define CBAR() asm volatile("" ::: "memory")
#define SB() __builtin_amdgcn_sched_barrier(0)

__device__ __forceinline__ u64 rt() { SB(); u64 t; asm volatile("s_memrealtime %0\n\ts_waitcnt lgkmcnt(0)" : "=s"(t)::"memory"); SB(); return t; }
#define STAMP(i) do { ts[i] = rt(); } while (0)
#define STAMP_DECL u64 ts[12] = {0,0,0,0,0,0,0,0,0,0,0,0}
#define STAMP_FLUSH() do { if (b == PROBE_B && wave == PROBE_W && lane == 0) { __hip_atomic_store((GAS u64*)(p.ws + WS_DIAG), rt(), __ATOMIC_RELAXED, __HIP_MEMORY_SCOPE_AGENT); } } while (0)

struct Lds {
  float qp[4][64];
  float gp[4][128];
  u32 qb[256];
  float op[4][512];
  float ml[4][4][2];
  float coef[24];
  u32 tag;
  u32 f_qp, f_gp, f_q, f_op, f_coef, f_visd, f_tag;
};

__device__ __forceinline__ void lds_wait(volatile LAS u32* f, u32 want) {
#pragma unroll 1
  for (u32 s = 0; s < SPIN_CAP; ++s) {
    if (*f >= want) break;
    __builtin_amdgcn_s_sleep(1);
  }
  CBAR();
}
__device__ __forceinline__ void lds_post(LAS u32* f, int lane) {
  CBAR();
  if (lane == 0) __hip_atomic_fetch_add(f, 1u, __ATOMIC_RELAXED, __HIP_MEMORY_SCOPE_WORKGROUP);
  CBAR();
}

__device__ __forceinline__ void ln_stats(u32x4 xv, float& m, float& sd) {
  float e[8];
#pragma unroll
  for (int k = 0; k < 4; ++k) { e[2 * k] = bf_lo(xv[k]); e[2 * k + 1] = bf_hi(xv[k]); }
  float s = ((e[0] + e[1]) + (e[2] + e[3])) + ((e[4] + e[5]) + (e[6] + e[7]));
  const float mean = wave_sum(s) * (1.f / DM);
  float v = 0.f;
#pragma unroll
  for (int k = 0; k < 8; ++k) { const float d = e[k] - mean; v = fmaf(d, d, v); }
  const float var = wave_sum(v) * (1.f / DM);
  m = rbf(mean);
  sd = rbf(sqrtf(rbf(rbf(var) + 1e-5f)));
}
__device__ __forceinline__ float ln_apply(u16 xr, float m, float sd) {
  return rbf(rbf(__uint_as_float((u32)xr << 16) - m) / sd);
}
__device__ __forceinline__ float tred8(const float (&a)[8], int lane) {
  const float b0 = tr32(a[0], a[4]), b1 = tr32(a[1], a[5]), b2 = tr32(a[2], a[6]), b3 = tr32(a[3], a[7]);
  const float c0 = tr16(b0, b2), c1 = tr16(b1, b3);
  const float d0 = c0 + dpp<XOR8>(c0), d1 = c1 + dpp<XOR8>(c1);
  return (lane & 8) ? d1 : d0;
}

__global__ void __launch_bounds__(NTHR) fused_decode(Params p) {
  __shared__ __attribute__((aligned(16))) Lds sm_;
  LAS Lds* sm = (LAS Lds*)&sm_;
  const int b = blockIdx.x, tid = threadIdx.x, lane = tid & 63;
  const int wave = __builtin_amdgcn_readfirstlane(tid >> 6);
  const int h = b & 7, j = b >> 3;
  const __amdgpu_buffer_rsrc_t wsr = __builtin_amdgcn_make_buffer_rsrc(p.ws, 0, WS_END, 0x00020000);
  STAMP_DECL;
  STAMP(0);

  if (wave < 4) {
    const int w = wave;
    const u32x4 xv = *(const u32x4*)(p.x + lane * 8);
    const int rg = j & 3, cg = j >> 2;
    u16 xr[4]; u32x4 wq[4];
#pragma unroll
    for (int i = 0; i < 4; ++i) xr[i] = p.x[rg * 128 + w * 32 + i * 8 + (lane >> 3)];
#pragma unroll
    for (int i = 0; i < 4; ++i) wq[i] = ldnt(p.wq + (size_t)(rg * 128 + w * 32 + i * 8 + (lane >> 3)) * (HQ * DH) + h * 512 + cg * 64 + (lane & 7) * 8);
    const size_t kvoff = (size_t)h * p.kv_head_stride + p.kv_win_off + (size_t)(j * 128 + w * 32) * DH;
    const u16* Kb = p.K + kvoff; const u16* Vb = p.V + kvoff;
    u32x4 kf[8], vf[8];
#pragma unroll
    for (int i = 0; i < 8; ++i) kf[i] = ldnt(Kb + ((i >> 2) * 16 + (lane & 15)) * DH + (i & 3) * 32 + (lane >> 4) * 8);
    SB();
    __builtin_amdgcn_s_barrier();
    SB();
    float m, sd; ln_stats(xv, m, sd);
    STAMP(1);
    float a[8];
#pragma unroll
    for (int e = 0; e < 8; ++e) a[e] = 0.f;
#pragma unroll
    for (int i = 0; i < 4; ++i) {
      const float xn = ln_apply(xr[i], m, sd);
#pragma unroll
      for (int k = 0; k < 4; ++k) { a[2 * k] = fmaf(xn, bf_lo(wq[i][k]), a[2 * k]); a[2 * k + 1] = fmaf(xn, bf_hi(wq[i][k]), a[2 * k + 1]); }
    }
    const float qv = tred8(a, lane);
    sm->qp[w][(lane & 7) * 8 + (lane >> 3)] = qv;
    lds_post(&sm->f_qp, lane);
    STAMP(2);
    SB();
#pragma unroll
    for (int i = 0; i < 8; ++i) vf[i] = ldnt(Vb + ((i >> 2) * 16 + (lane >> 4) * 4 + (i & 3)) * DH + (lane & 15) * 8);
    SB();
    lds_post(&sm->f_visd, lane);
    STAMP(3);
    lds_wait(&sm->f_q, 1);
    STAMP(4);
    u32x4 qf[4];
#pragma unroll
    for (int ii = 0; ii < 4; ++ii) qf[ii] = *(const LAS u32x4*)&sm->qb[(((lane & 15) >> 2) * 128 + ii * 32 + (lane >> 4) * 8) >> 1];
    f32x4 s[2];
#pragma unroll
    for (int t = 0; t < 2; ++t) {
      s[t] = f32x4{0.f, 0.f, 0.f, 0.f};
#pragma unroll
      for (int ii = 0; ii < 4; ++ii)
        s[t] = __builtin_amdgcn_mfma_f32_16x16x32_bf16(__builtin_bit_cast(bf16x8, kf[t * 4 + ii]), __builtin_bit_cast(bf16x8, qf[ii]), s[t], 0, 0, 0);
    }
    float sc[8];
#pragma unroll
    for (int k = 0; k < 8; ++k) sc[k] = rbf(rbf(s[k >> 2][k & 3]) * 0.08838834764831845f) * 1.4426950408889634f;
    float mx = fmaxf(fmaxf(fmaxf(sc[0], sc[1]), fmaxf(sc[2], sc[3])), fmaxf(fmaxf(sc[4], sc[5]), fmaxf(sc[6], sc[7])));
    mx = max32(max16(mx));
    float pr[8], ls = 0.f;
#pragma unroll
    for (int k = 0; k < 8; ++k) { pr[k] = __builtin_amdgcn_exp2f(sc[k] - mx); ls += pr[k]; }
    ls = sum32(sum16(ls));
    if (lane < 16 && (lane & 3) == 0) *(LAS f32x2*)&sm->ml[w][lane >> 2][0] = f32x2{mx, ls};
    const u32x4 pf = {pk_bf16(pr[0], pr[1]), pk_bf16(pr[2], pr[3]), pk_bf16(pr[4], pr[5]), pk_bf16(pr[6], pr[7])};
    STAMP(5);
    float o[8];
#pragma unroll
    for (int e = 0; e < 8; ++e) {
      const u32 sel = (e & 1) ? 0x07060302u : 0x05040100u;
      u32x4 bv;
#pragma unroll
      for (int k = 0; k < 4; ++k) bv[k] = __builtin_amdgcn_perm(vf[2 * k + 1][e >> 1], vf[2 * k][e >> 1], sel);
      const f32x4 d = __builtin_amdgcn_mfma_f32_16x16x32_bf16(__builtin_bit_cast(bf16x8, pf), __builtin_bit_cast(bf16x8, bv), f32x4{0.f, 0.f, 0.f, 0.f}, 0, 0, 0);
      o[e] = d[0];
    }
    {
      LAS float* dst = &sm->op[w][(lane >> 4) * 128 + (lane & 15) * 8];
      *(LAS f32x4*)dst = f32x4{o[0], o[1], o[2], o[3]};
      *(LAS f32x4*)(dst + 4) = f32x4{o[4], o[5], o[6], o[7]};
    }
    lds_post(&sm->f_op, lane);
    STAMP(6);
    lds_wait(&sm->f_op, 4);
    {
      const u32 tag = sm->tag;
      float mw[4], lw[4]; f32x2 ov[4];
#pragma unroll
      for (int k = 0; k < 4; ++k) { const f32x2 t = *(const LAS f32x2*)&sm->ml[k][w][0]; mw[k] = t[0]; lw[k] = t[1]; ov[k] = *(const LAS f32x2*)&sm->op[k][w * 128 + 2 * lane]; }
      const float M = fmaxf(fmaxf(mw[0], mw[1]), fmaxf(mw[2], mw[3]));
      float L = 0.f, o0 = 0.f, o1 = 0.f;
#pragma unroll
      for (int k = 0; k < 4; ++k) { const float wt = __builtin_amdgcn_exp2f(mw[k] - M); L = fmaf(wt, lw[k], L); o0 = fmaf(wt, ov[k][0], o0); o1 = fmaf(wt, ov[k][1], o1); }
      const u32x4 g = {__float_as_uint(o0), tag, __float_as_uint(o1), tag};
      __builtin_amdgcn_raw_buffer_store_b128(g, wsr, WS_OS + (u32)(((h * 32 + w * 8 + (lane >> 3)) * 32 + j) * 128 + (lane & 7) * 16), 0, 16);
      if (lane == 0) {
        const u32x4 gm = {__float_as_uint(M), tag, __float_as_uint(L), tag};
        __builtin_amdgcn_raw_buffer_store_b128(gm, wsr, WS_ML + (u32)(((h * 4 + w) * 32 + j) * 16), 0, 16);
      }
    }
    STAMP(7);
  } else if (wave < 8) {
    const int w = wave - 4;
    const bool ffn = b < 224;
    const int bb = ffn ? b : 0;
    const int lc = bb % 28, rg = bb / 28;
    const u32x4 xv = *(const u32x4*)(p.x + lane * 8);
    u16 xr[2];
#pragma unroll
    for (int i = 0; i < 2; ++i) xr[i] = p.x[rg * 64 + w * 16 + i * 8 + (lane >> 3)];
    SB();
    __builtin_amdgcn_s_barrier();
    SB();
    float m, sd; ln_stats(xv, m, sd);
    float xn[2];
#pragma unroll
    for (int i = 0; i < 2; ++i) xn[i] = ln_apply(xr[i], m, sd);
    STAMP(1);
    lds_wait(&sm->f_visd, 4);
    STAMP(2);
    u32x4 g1[2], g3[2], wo[6];
    if (ffn) {
#pragma unroll
      for (int i = 0; i < 2; ++i) {
        const size_t off = (size_t)(rg * 64 + w * 16 + i * 8 + (lane >> 3)) * FF + lc * 64 + (lane & 7) * 8;
        g1[i] = ldnt(p.w1 + off); g3[i] = ldnt(p.w3 + off);
      }
    }
    const int orow = h * 512 + j * 16;
#pragma unroll
    for (int i = 0; i < 4; ++i) wo[i] = ldnt(p.wo + (size_t)(orow + i * 4 + (lane >> 4)) * DM + w * 128 + (lane & 15) * 8);
#pragma unroll
    for (int i = 0; i < 2; ++i) wo[4 + i] = ldnt(p.w2 + (size_t)(bb * 8 + i * 4 + (lane >> 4)) * DM + w * 128 + (lane & 15) * 8);
    SB();
    STAMP(3);
    if (ffn) {
      float a[8], c[8];
#pragma unroll
      for (int e = 0; e < 8; ++e) { a[e] = 0.f; c[e] = 0.f; }
#pragma unroll
      for (int i = 0; i < 2; ++i)
#pragma unroll
        for (int k = 0; k < 4; ++k) {
          a[2 * k] = fmaf(xn[i], bf_lo(g1[i][k]), a[2 * k]); a[2 * k + 1] = fmaf(xn[i], bf_hi(g1[i][k]), a[2 * k + 1]);
          c[2 * k] = fmaf(xn[i], bf_lo(g3[i][k]), c[2 * k]); c[2 * k + 1] = fmaf(xn[i], bf_hi(g3[i][k]), c[2 * k + 1]);
        }
      const float gv = tred8(a, lane), uv = tred8(c, lane);
      *(LAS f32x2*)&sm->gp[w][((lane & 7) * 8 + (lane >> 3)) * 2] = f32x2{gv, uv};
      lds_post(&sm->f_gp, lane);
      if (w == 0) {
        lds_wait(&sm->f_gp, 4);
        lds_wait(&sm->f_tag, 1);
        const u32 tag = sm->tag;
        f32x2 t = *(const LAS f32x2*)&sm->gp[0][lane * 2];
#pragma unroll
        for (int k = 1; k < 4; ++k) t += *(const LAS f32x2*)&sm->gp[k][lane * 2];
        const u32x4 g = {__float_as_uint(t[0]), tag, __float_as_uint(t[1]), tag};
        __builtin_amdgcn_raw_buffer_store_b128(g, wsr, WS_GS + (u32)((((lc * 8 + (lane >> 3)) * 8 + rg) * 8 + (lane & 7)) * 16), 0, 16);
      }
    }
    STAMP(4);
    float wf[6][8];
#pragma unroll
    for (int i = 0; i < 6; ++i)
#pragma unroll
      for (int k = 0; k < 4; ++k) { wf[i][2 * k] = bf_lo(wo[i][k]); wf[i][2 * k + 1] = bf_hi(wo[i][k]); }
#pragma unroll
    for (int i = 0; i < 6; ++i)
#pragma unroll
      for (int k = 0; k < 8; ++k) asm volatile("" : "+v"(wf[i][k]));
    STAMP(5);
    lds_wait(&sm->f_coef, 1);
    STAMP(6);
    const u32 tag = sm->tag;
    float a[8];
#pragma unroll
    for (int e = 0; e < 8; ++e) a[e] = 0.f;
#pragma unroll
    for (int i = 0; i < 6; ++i) {
      const float cf = sm->coef[i * 4 + (lane >> 4)];
#pragma unroll
      for (int e = 0; e < 8; ++e) a[e] = fmaf(cf, wf[i][e], a[e]);
    }
    const float b0 = tr32(a[0], a[4]), b1 = tr32(a[1], a[5]), b2 = tr32(a[2], a[6]), b3 = tr32(a[3], a[7]);
    const float c0 = tr16(b0, b2), c1 = tr16(b1, b3);
    const u32x4 g = {__float_as_uint(c0), tag, __float_as_uint(c1), tag};
    __builtin_amdgcn_raw_buffer_store_b128(g, wsr, WS_PS + (u32)((b * 256 + w * 64 + (lane & 15) * 4 + (lane >> 5) * 2 + ((lane >> 4) & 1)) * 16), 0, 16);
    STAMP(7);
  } else {
    const u32 ep = __builtin_amdgcn_raw_buffer_load_b32(wsr, WS_EPOCH, 0, 16);
    const int cp = (b & 7) * 32 + (b >> 3);
    const u32 xo = *(const u32*)(p.x + 2 * cp);
    if (lane < 7) (&sm->f_qp)[lane] = 0u;
    asm volatile("s_waitcnt lgkmcnt(0)" ::: "memory");
    SB();
    __builtin_amdgcn_s_barrier();
    SB();
    const u32 tag = ep * 1664525u + 1013904223u;
    if (lane == 0) sm->tag = tag;
    lds_post(&sm->f_tag, lane);
    STAMP(1);
    lds_wait(&sm->f_qp, 4);
    {
      const int rg = j & 3, cg = j >> 2;
      if (lane < 32) {
        f32x2 t = *(const LAS f32x2*)&sm->qp[0][lane * 2];
#pragma unroll
        for (int k = 1; k < 4; ++k) t += *(const LAS f32x2*)&sm->qp[k][lane * 2];
        const u32x4 g = {__float_as_uint(t[0]), tag, __float_as_uint(t[1]), tag};
        __builtin_amdgcn_raw_buffer_store_b128(g, wsr, WS_QS + (u32)(((h * 4 + rg) * 256 + cg * 32 + lane) * 16), 0, 16);
      }
    }
    STAMP(2);
    {
      u32x4 g[16];
#pragma unroll 1
      for (u32 s = 0; s < SPIN_CAP; ++s) {
        CBAR();
        bool ok = true;
#pragma unroll
        for (int k = 0; k < 16; ++k) g[k] = __builtin_amdgcn_raw_buffer_load_b128(wsr, WS_QS + (u32)((h * 1024 + k * 64 + lane) * 16), 0, 16);
#pragma unroll
        for (int k = 0; k < 16; ++k) ok &= (g[k][1] == tag) & (g[k][3] == tag);
        if (__all(ok)) break;
        __builtin_amdgcn_s_sleep(2);
      }
#pragma unroll
      for (int i = 0; i < 4; ++i) {
        const float q0 = (__uint_as_float(g[i][0]) + __uint_as_float(g[4 + i][0])) + (__uint_as_float(g[8 + i][0]) + __uint_as_float(g[12 + i][0]));
        const float q1 = (__uint_as_float(g[i][2]) + __uint_as_float(g[4 + i][2])) + (__uint_as_float(g[8 + i][2]) + __uint_as_float(g[12 + i][2]));
        sm->qb[i * 64 + lane] = pk_bf16(q0, q1);
      }
      lds_post(&sm->f_q, lane);
    }
    STAMP(3);
    lds_wait(&sm->f_op, 4);
    STAMP(4);
    {
      const bool ffn = b < 224;
      u32x4 go[4], gm[4], gg;
#pragma unroll 1
      for (u32 s = 0; s < SPIN_CAP; ++s) {
        CBAR();
        bool ok = true;
#pragma unroll
        for (int k = 0; k < 4; ++k) go[k] = __builtin_amdgcn_raw_buffer_load_b128(wsr, WS_OS + (u32)(((h * 32 + j) * 32) * 128 + (k * 64 + lane) * 16), 0, 16);
#pragma unroll
        for (int k = 0; k < 4; ++k) gm[k] = __builtin_amdgcn_raw_buffer_load_b128(wsr, WS_ML + (u32)(((h * 4 + (j >> 3)) * 32 + k * 8 + (lane >> 3)) * 16), 0, 16);
        gg = __builtin_amdgcn_raw_buffer_load_b128(wsr, WS_GS + (u32)(((ffn ? b : 0) * 64 + lane) * 16), 0, 16);
#pragma unroll
        for (int k = 0; k < 4; ++k) ok &= (go[k][1] == tag) & (go[k][3] == tag) & (gm[k][1] == tag) & (gm[k][3] == tag);
        ok &= (gg[1] == tag) & (gg[3] == tag);
        if (__all(ok)) break;
        __builtin_amdgcn_s_sleep(1);
      }
      STAMP(5);
      float M = fmaxf(fmaxf(__uint_as_float(gm[0][0]), __uint_as_float(gm[1][0])), fmaxf(__uint_as_float(gm[2][0]), __uint_as_float(gm[3][0])));
      M = fmaxf(M, dpp<XOR8>(M)); M = max32(max16(M));
      float L = 0.f, o0 = 0.f, o1 = 0.f;
#pragma unroll
      for (int k = 0; k < 4; ++k) {
        const float wt = __builtin_amdgcn_exp2f(__uint_as_float(gm[k][0]) - M);
        L = fmaf(wt, __uint_as_float(gm[k][2]), L); o0 = fmaf(wt, __uint_as_float(go[k][0]), o0); o1 = fmaf(wt, __uint_as_float(go[k][2]), o1);
      }
      float gt = __uint_as_float(gg[0]), up = __uint_as_float(gg[2]);
      L += dpp<XOR8>(L); o0 += dpp<XOR8>(o0); o1 += dpp<XOR8>(o1); gt += dpp<XOR8>(gt); up += dpp<XOR8>(up);
      L = sum32(sum16(L)); o0 = sum32(sum16(o0)); o1 = sum32(sum16(o1)); gt = sum32(sum16(gt)); up = sum32(sum16(up));
      const float inv = 1.f / L;
      gt = rbf(gt); up = rbf(up);
      const float sl = rbf(gt / (1.f + __expf(-gt)));
      const float hv = ffn ? rbf(sl * up) : 0.f;
      if (lane < 8) { *(LAS f32x2*)&sm->coef[lane * 2] = f32x2{rbf(o0 * inv), rbf(o1 * inv)}; sm->coef[16 + lane] = hv; }
      lds_post(&sm->f_coef, lane);
    }
    STAMP(6);
    {
      u32x4 g[4];
#pragma unroll 1
      for (u32 s = 0; s < SPIN_CAP; ++s) {
        CBAR();
        bool ok = true;
#pragma unroll
        for (int k = 0; k < 4; ++k) g[k] = __builtin_amdgcn_raw_buffer_load_b128(wsr, WS_PS + (u32)(((k * 64 + lane) * 256 + cp) * 16), 0, 16);
#pragma unroll
        for (int k = 0; k < 4; ++k) ok &= (g[k][1] == tag) & (g[k][3] == tag);
        if (__all(ok)) break;
        __builtin_amdgcn_s_sleep(1);
      }
      STAMP(7);
      float s0 = (__uint_as_float(g[0][0]) + __uint_as_float(g[1][0])) + (__uint_as_float(g[2][0]) + __uint_as_float(g[3][0]));
      float s1 = (__uint_as_float(g[0][2]) + __uint_as_float(g[1][2])) + (__uint_as_float(g[2][2]) + __uint_as_float(g[3][2]));
      s0 = wave_sum(s0); s1 = wave_sum(s1);
      if (lane == 0) {
        *(u32*)(p.out + 2 * cp) = pk_bf16(bf_lo(xo) + s0, bf_hi(xo) + s1);
        if (b == 0) __builtin_amdgcn_raw_buffer_store_b32(tag, wsr, WS_EPOCH, 0, 16);
      }
      STAMP(8);
    }
  }
  STAMP_FLUSH();
}
__global__ void tail_probe(unsigned char* ws, int mult) {
  const u64 t0 = __builtin_amdgcn_s_memrealtime();
  SB();
  const u64 te = __hip_atomic_load((GAS u64*)(ws + WS_DIAG), __ATOMIC_RELAXED, __HIP_MEMORY_SCOPE_AGENT);
  u64 gap = t0 - te; if (gap > 100000ull) gap = 100000ull;
  const u64 until = rt() + (u64)mult * gap; while (rt() < until) __builtin_amdgcn_s_sleep(4);
}
__global__ void head_probe(unsigned char* ws) {
  if (threadIdx.x == 0 && blockIdx.x == 0) __hip_atomic_store((GAS u64*)(ws + WS_DIAG + 256), rt(), __ATOMIC_RELAXED, __HIP_MEMORY_SCOPE_AGENT);
}
}

extern "C" void kernel_launch(void* const* d_in, const int* in_sizes, int n_in, void* d_out, int out_size, void* d_ws,
                              size_t ws_size, hipStream_t stream) {
  Params p;
  p.x = (const u16*)d_in[0]; p.K = (const u16*)d_in[1]; p.V = (const u16*)d_in[2];
  p.wq = (const u16*)d_in[3]; p.wo = (const u16*)d_in[4];
  p.w1 = (const u16*)d_in[6]; p.w3 = (const u16*)d_in[7]; p.w2 = (const u16*)d_in[8];
  p.out = (u16*)d_out; p.ws = (unsigned char*)d_ws;
  const u64 sk = (u64)in_sizes[1] / (HKV * DH);
  p.kv_head_stride = sk * DH;
  p.kv_win_off = (sk - WIN) * DH;
  hipLaunchKernelGGL(head_probe, dim3(1), dim3(64), 0, stream, p.ws);
  hipLaunchKernelGGL(fused_decode, dim3(NWG), dim3(NTHR), 0, stream, p);
}
```

```cpp
#define DIAG 1
#include <hip/hip_runtime.h>
#include <stdint.h>

typedef unsigned short u16;
typedef unsigned u32;
typedef unsigned long long u64;
typedef u32 u32x4 __attribute__((ext_vector_type(4)));
typedef u32 u32x2 __attribute__((ext_vector_type(2)));
typedef float f32x4 __attribute__((ext_vector_type(4)));
typedef float f32x2 __attribute__((ext_vector_type(2)));
typedef short s16x4 __attribute__((ext_vector_type(4)));
typedef __bf16 bf16x8 __attribute__((ext_vector_type(8)));
typedef __bf16 bf16x2 __attribute__((ext_vector_type(2)));
#define LAS __attribute__((address_space(3)))
#define GAS __attribute__((address_space(1)))

#define DEPTH 6

namespace {
constexpr int DM = 512, HQ = 32, HKV = 8, DH = 128, WIN = 4096, FF = 1792;
constexpr int NWG = 256, NTHR = 640;
constexpr u32 WS_EPOCH = 0;
constexpr u32 WS_QS = 4096;
constexpr u32 WS_ML = WS_QS + 8 * 4 * 512 * 8;
constexpr u32 WS_GS = WS_ML + 256 * 128;
constexpr u32 WS_OS = WS_GS + 8 * 1792 * 16;
constexpr u32 WS_PS = WS_OS + 8 * 16 * 32 * 128;
constexpr u32 WS_END = WS_PS + 32 * 128 * 128;
constexpr u32 WS_DIAG = 8u << 20;
constexpr u32 SPIN_CAP = 1u << 22;
constexpr u32 L_WQ = 0, L_K = 16384, L_V = L_K + 32768, L_W13 = L_V + 32768, L_WO = L_W13 + 16384, L_W2 = L_WO + 16384;
constexpr u32 L_XN = L_W2 + 8192, L_Q = L_XN + 1024, L_P = L_Q + 1024, L_ML = L_P + 4096, L_CO = L_ML + 128, L_CH = L_CO + 64;
constexpr u32 L_FIN = L_CH + 64, L_FLG = L_FIN + 512, L_END = L_FLG + 128;
enum { F_PRG = 0, F_P = 4, F_FIN = 8, F_Q = 16, F_TAG = 20, F_O, F_H, F_TAGV };
enum { G_WQ = 1, G_K, G_V0, G_V1, G_V2, G_V3, G_W13, G_ALL };

struct Params {
  const u16 *x, *K, *V, *wq, *wo, *w1, *w3, *w2;
  u16* out;
  unsigned char* ws;
  u64 kv_head_stride;
  u64 kv_win_off;
};

__device__ __forceinline__ float bf_lo(u32 v) { return __uint_as_float(v << 16); }
__device__ __forceinline__ float bf_hi(u32 v) { return __uint_as_float(v & 0xffff0000u); }
__device__ __forceinline__ u32 pk_bf16(float lo, float hi) {
  bf16x2 r = {(__bf16)lo, (__bf16)hi};
  return __builtin_bit_cast(u32, r);
}
__device__ __forceinline__ u32 cvtpk(float lo, float hi) {
  u32 r = __builtin_bit_cast(u32, __builtin_convertvector(f32x2{lo, hi}, bf16x2));
  asm("" : "+v"(r));
  return r;
}
__device__ __forceinline__ float max2(float a, float b) { return __builtin_elementwise_maximum(a, b); }
__device__ __forceinline__ float max3(float a, float b, float c) { return max2(max2(a, b), c); }
__device__ __forceinline__ float rbf(float f) { return (float)(__bf16)f; }
template <int CTRL> __device__ __forceinline__ float dpp(float x) {
  return __builtin_bit_cast(float, __builtin_amdgcn_mov_dpp(__builtin_bit_cast(int, x), CTRL, 0xf, 0xf, true));
}
constexpr int XOR1 = 0xB1, XOR2 = 0x4E, XOR7 = 0x141, XOR8 = 0x128;
__device__ __forceinline__ float tr32(float a, float b) {
  auto s = __builtin_amdgcn_permlane32_swap(__float_as_uint(a), __float_as_uint(b), false, false);
  return __uint_as_float(s[0]) + __uint_as_float(s[1]);
}
__device__ __forceinline__ float tr16(float a, float b) {
  auto s = __builtin_amdgcn_permlane16_swap(__float_as_uint(a), __float_as_uint(b), false, false);
  return __uint_as_float(s[0]) + __uint_as_float(s[1]);
}
__device__ __forceinline__ float sum32(float x) { return tr32(x, x); }
__device__ __forceinline__ float sum16(float x) { return tr16(x, x); }
__device__ __forceinline__ float max32(float x) {
  auto s = __builtin_amdgcn_permlane32_swap(__float_as_uint(x), __float_as_uint(x), false, false);
  return max2(__uint_as_float(s[0]), __uint_as_float(s[1]));
}
__device__ __forceinline__ float max16(float x) {
  auto s = __builtin_amdgcn_permlane16_swap(__float_as_uint(x), __float_as_uint(x), false, false);
  return max2(__uint_as_float(s[0]), __uint_as_float(s[1]));
}
__device__ __forceinline__ float row_sum(float x) {
  x += dpp<XOR1>(x); x += dpp<XOR2>(x); x += dpp<XOR7>(x); x += dpp<XOR8>(x);
  return x;
}
__device__ __forceinline__ float row_max(float x) {
  x = fmaxf(x, dpp<XOR1>(x)); x = fmaxf(x, dpp<XOR2>(x)); x = fmaxf(x, dpp<XOR7>(x)); x = fmaxf(x, dpp<XOR8>(x));
  return x;
}
__device__ __forceinline__ float wave_sum(float x) { return sum32(sum16(row_sum(x))); }
#define CBAR() asm volatile("" ::: "memory")
#define SB() __builtin_amdgcn_sched_barrier(0)

__device__ __forceinline__ u64 rt() { SB(); u64 t; asm volatile("s_memrealtime %0\n\ts_waitcnt lgkmcnt(0)" : "=s"(t)::"memory"); SB(); return t; }
#define STAMP(i) do { ts[i] = rt(); } while (0)
#define STAMP_DECL u64 ts[12] = {0,0,0,0,0,0,0,0,0,0,0,0}
#define STAMP_FLUSH() do { if (lane == 0) { _Pragma("unroll") for (int i_ = 0; i_ < 12; ++i_) ((u64*)(p.ws + WS_DIAG))[((size_t)b * 16 + wave) * 16 + i_] = ts[i_]; } } while (0)

typedef LAS unsigned char* lptr;
#define PIN(x) asm volatile("" : "+v"(x))
template <int SLEEP> __device__ __forceinline__ void lds_wait(u32 fb, int f, u32 want) {
  u32 v, cnt = SPIN_CAP; u64 m;
  asm volatile(
      "1:\n\tds_read_b32 %0, %3 offset:%4\n\ts_sub_u32 %1, %1, 1\n\ts_cselect_b64 %2, 0, -1\n\ts_waitcnt lgkmcnt(0)\n\t"
      "v_cmp_gt_u32 vcc, %5, %0\n\ts_and_b64 vcc, vcc, %2\n\ts_cbranch_vccz 2f\n\ts_sleep %6\n\ts_branch 1b\n2:"
      : "=&v"(v), "+s"(cnt), "=&s"(m) : "v"(fb), "i"(f * 4), "i"(want), "i"(SLEEP) : "vcc", "scc", "memory");
}
template <> __device__ __forceinline__ void lds_wait<0>(u32 fb, int f, u32 want) {
  u32 v, cnt = SPIN_CAP; u64 m;
  asm volatile(
      "1:\n\tds_read_b32 %0, %3 offset:%4\n\ts_sub_u32 %1, %1, 1\n\ts_cselect_b64 %2, 0, -1\n\ts_waitcnt lgkmcnt(0)\n\t"
      "v_cmp_gt_u32 vcc, %5, %0\n\ts_and_b64 vcc, vcc, %2\n\ts_cbranch_vccnz 1b"
      : "=&v"(v), "+s"(cnt), "=&s"(m) : "v"(fb), "i"(f * 4), "i"(want) : "vcc", "scc", "memory");
}
__device__ __forceinline__ void lds_post(u32 addr, u32 val, int f) {
  asm volatile("s_mov_b64 exec, 1\n\tds_write_b32 %0, %1 offset:%2\n\ts_mov_b64 exec, -1" ::"v"(addr), "v"(val), "i"(f * 4) : "memory");
}
__device__ __forceinline__ u32x4 make_srd(const void* base, u32 bytes) {
  const u64 a = (u64)base;
  return u32x4{(u32)a, (u32)(a >> 32) & 0xffffu, bytes, 0x00020000u};
}
#define DMA(srd, voff, soff, ldsa) \
  asm volatile("s_mov_b32 m0, %3\n\ts_nop 0\n\tbuffer_load_dwordx4 %1, %0, %2 offen nt lds" ::"s"(srd), "v"(voff), "s"(soff), "s"(ldsa) : "memory")
#define VMW(n) asm volatile("s_waitcnt vmcnt(%0)" ::"i"(n) : "memory")

__device__ __forceinline__ u32x2 trr(lptr a) {
  typedef short v4s __attribute__((ext_vector_type(4)));
  return __builtin_bit_cast(u32x2, __builtin_amdgcn_ds_read_tr16_b64_v4i16((LAS v4s*)a));
}
__device__ __forceinline__ f32x4 mfma32(u32x4 a, u32x4 b, f32x4 c) {
  return __builtin_amdgcn_mfma_f32_16x16x32_bf16(__builtin_bit_cast(bf16x8, a), __builtin_bit_cast(bf16x8, b), c, 0, 0, 0);
}
__device__ __forceinline__ f32x4 mfma16(u32x2 a, u32x2 b, f32x4 c) {
  return __builtin_amdgcn_mfma_f32_16x16x16bf16_1k(__builtin_bit_cast(s16x4, a), __builtin_bit_cast(s16x4, b), c, 0, 0, 0);
}
__device__ __forceinline__ u32x4 cat(u32x2 a, u32x2 b) { return u32x4{a[0], a[1], b[0], b[1]}; }
__device__ __forceinline__ bool tagged(u32x4 g, u32 tag) { return (g[1] == tag) & (g[3] == tag); }

__device__ __forceinline__ u32 bad_acc(u32 acc, u32x4 g, u32 tag) { return acc | (g[1] ^ tag) | (g[3] ^ tag); }
#define WS_LD(off) __builtin_amdgcn_raw_buffer_load_b128(wsr, (off), 0, 16)
#define WS_ST(v, off) __builtin_amdgcn_raw_buffer_store_b128((v), wsr, (off), 0, 16)
#define POLL(N, dst, off, stride) \
  _Pragma("unroll 1") for (u32 s_ = 0; s_ < SPIN_CAP; ++s_) { \
    CBAR(); \
    _Pragma("unroll") for (int k_ = 0; k_ < (N); ++k_) dst[k_] = WS_LD((off) + k_ * (stride)); \
    u32 bad_ = 0; \
    _Pragma("unroll") for (int k_ = 0; k_ < (N); ++k_) bad_ = bad_acc(bad_, dst[k_], tag); \
    if (__builtin_amdgcn_ballot_w64(bad_ != 0) == 0) break; \
  }
#define SLEEP_Q 16
#define SLEEP_O 8
#define SLEEP_G 8

__global__ void __launch_bounds__(NTHR) fused_decode(Params p) {
  extern __shared__ __attribute__((aligned(16))) unsigned char smem[];
  lptr lds = (lptr)smem;
  const u32 lbase = (u32)(uintptr_t)lds;
  const int b = blockIdx.x, tid = threadIdx.x, lane = tid & 63;
  const int wave = __builtin_amdgcn_readfirstlane(tid >> 6);
  const int h = b & 7, j = b >> 3;
  const int g = lane >> 4, c = lane & 15;
  const bool ffn1 = b < 224;
  const int org = j & 15, ocg = j >> 4;
  const __amdgpu_buffer_rsrc_t wsr = __builtin_amdgcn_make_buffer_rsrc(p.ws, 0, WS_END, 0x00020000);
  const u32 fb = lbase + L_FLG, fb4 = fb + (lane & 3) * 4, one = 1u;
  const int trow = 4 * g + (c >> 2);
  STAMP_DECL;
  STAMP(0);

  if (wave == 0) {
    const u32x4 xv = *(const u32x4*)(p.x + lane * 8);
    const u32 ep = __builtin_amdgcn_raw_buffer_load_b32(wsr, WS_EPOCH, 0, 16);
    __builtin_amdgcn_s_setprio(3);
    if (lane < 32) *(LAS u32*)(lds + L_FLG + lane * 4) = 0u;
    asm volatile("s_waitcnt lgkmcnt(0)" ::: "memory");
    SB();
    __builtin_amdgcn_s_barrier();
    SB();
    float xe[8];
#pragma unroll
    for (int k = 0; k < 4; ++k) { xe[2 * k] = bf_lo(xv[k]); xe[2 * k + 1] = bf_hi(xv[k]); }
    const float mean = wave_sum(((xe[0] + xe[1]) + (xe[2] + xe[3])) + ((xe[4] + xe[5]) + (xe[6] + xe[7]))) * (1.f / DM);
    float v = 0.f;
#pragma unroll
    for (int k = 0; k < 8; ++k) { const float d = xe[k] - mean; v = fmaf(d, d, v); }
    const float var = wave_sum(v) * (1.f / DM);
    const float m = rbf(mean);
    const float rs = __builtin_amdgcn_rcpf(rbf(__builtin_amdgcn_sqrtf(rbf(rbf(var) + 1e-5f))));
    u32 xn[4];
#pragma unroll
    for (int k = 0; k < 4; ++k) { const u32 d = cvtpk(xe[2 * k] - m, xe[2 * k + 1] - m); xn[k] = cvtpk(bf_lo(d) * rs, bf_hi(d) * rs); }
    lptr xd = lds + L_XN + (lane >> 2) * 64 + (lane & 1) * 32 + ((lane >> 1) & 1) * 8;
    *(LAS u32x2*)xd = u32x2{xn[0], xn[1]};
    *(LAS u32x2*)(xd + 16) = u32x2{xn[2], xn[3]};
    const u32 tag = ep * 1664525u + 1013904223u;
    *(LAS u32*)(lds + L_FLG + F_TAGV * 4) = tag;
    lds_post(fb, one, F_TAG);
    STAMP(1);
    u32 om = WS_ML + (u32)((h * 32 + (lane >> 3)) * 128 + (org >> 2) * 16);
    u32 oo = WS_OS + (u32)((h * 16 + org) * 4096 + lane * 16);
    u32 co = lbase + L_CO + (lane & 3) * 16 + ((lane >> 2) & 1) * 8 + (lane >> 5) * 4;
    PIN(om); PIN(oo); PIN(co);
    lds_wait<2>(fb4, F_P, 1);
    lds_wait<2>(fb4, F_PRG, G_V3);
    STAMP(2);
    float wt[4];
    {
      u32x4 gm[4];
      POLL(4, gm, om, 1024);
      STAMP(3);
      float M = max2(max3(__uint_as_float(gm[0][0]), __uint_as_float(gm[1][0]), __uint_as_float(gm[2][0])), __uint_as_float(gm[3][0]));
      M = max2(M, dpp<XOR8>(M)); M = max32(max16(M));
#pragma unroll
      for (int k = 0; k < 4; ++k) wt[k] = __builtin_amdgcn_exp2f(__uint_as_float(gm[k][0]) - M) * __uint_as_float(gm[k][2]);
      float L = (wt[0] + wt[1]) + (wt[2] + wt[3]);
      L += dpp<XOR8>(L); L = sum32(sum16(L));
      const float rl = __builtin_amdgcn_rcpf(L);
#pragma unroll
      for (int k = 0; k < 4; ++k) { wt[k] *= rl; PIN(wt[k]); }
    }
    STAMP(4);
    {
      u32x4 go[4];
      POLL(4, go, oo, 1024);
      STAMP(5);
      float a[4];
      a[0] = wt[0] * bf_lo(go[0][0]); a[1] = wt[0] * bf_hi(go[0][0]); a[2] = wt[0] * bf_lo(go[0][2]); a[3] = wt[0] * bf_hi(go[0][2]);
#pragma unroll
      for (int k = 1; k < 4; ++k) {
        a[0] = fmaf(wt[k], bf_lo(go[k][0]), a[0]); a[1] = fmaf(wt[k], bf_hi(go[k][0]), a[1]);
        a[2] = fmaf(wt[k], bf_lo(go[k][2]), a[2]); a[3] = fmaf(wt[k], bf_hi(go[k][2]), a[3]);
      }
      float b0 = tr32(a[0], a[2]), b1 = tr32(a[1], a[3]);
      b0 = sum16(b0); b1 = sum16(b1);
      b0 += dpp<XOR8>(b0); b1 += dpp<XOR8>(b1);
      *(LAS u32*)(uintptr_t)co = cvtpk(b0, b1);
      lds_post(fb, one, F_O);
    }
    STAMP(6);
  } else if (wave == 1) {
    __builtin_amdgcn_s_setprio(3);
    const int f0 = (h * 16 + org) * 14;
    u32 og = WS_GS + (u32)((g * FF + f0 + (c < 14 ? c : 13)) * 16);
    __builtin_amdgcn_s_barrier();
    lds_wait<2>(fb, F_TAG, 1);
    const u32 tag = *(LAS u32*)(lds + L_FLG + F_TAGV * 4);
    if (ffn1) {
      const int rg = b / 28;
      WS_ST((u32x4{0u, ~tag, 0u, ~tag}), WS_GS + (u32)((b * 64 + lane) * 16));
      u32x4 xf[2];
#pragma unroll
      for (int k = 0; k < 2; ++k) xf[k] = *(LAS u32x4*)(lds + L_XN + (rg * 2 + k) * 64 + g * 16);
      u32 wa[4];
#pragma unroll
      for (int n = 0; n < 4; ++n) { wa[n] = lbase + L_W13 + trow * 128 + ((n ^ ((trow >> 1) & 3)) * 32) + (c & 3) * 8; PIN(wa[n]); }
      PIN(og);
      lds_wait<1>(fb4, F_PRG, G_W13);
      STAMP(1);
      u32x2 wf[2][4][4];
#pragma unroll
      for (int m = 0; m < 2; ++m)
#pragma unroll
        for (int n = 0; n < 4; ++n)
#pragma unroll
          for (int q = 0; q < 4; ++q) wf[m][n][q] = trr((lptr)(uintptr_t)wa[n] + m * 8192 + q * 2048);
      SB();
      f32x4 t[2][4];
#pragma unroll
      for (int m = 0; m < 2; ++m)
#pragma unroll
        for (int n = 0; n < 4; ++n) t[m][n] = mfma32(xf[0], cat(wf[m][n][0], wf[m][n][1]), f32x4{0.f, 0.f, 0.f, 0.f});
#pragma unroll
      for (int m = 0; m < 2; ++m)
#pragma unroll
        for (int n = 0; n < 4; ++n) t[m][n] = mfma32(xf[1], cat(wf[m][n][2], wf[m][n][3]), t[m][n]);
      float r[2];
#pragma unroll
      for (int m = 0; m < 2; ++m) r[m] = (g & 2) ? ((g & 1) ? t[m][3][0] : t[m][2][0]) : ((g & 1) ? t[m][1][0] : t[m][0][0]);
      WS_ST((u32x4{__float_as_uint(r[0]), tag, __float_as_uint(r[1]), tag}), WS_GS + (u32)((b * 64 + lane) * 16));
    } else {
      lds_wait<1>(fb4, F_PRG, G_W13);
    }
    STAMP(2);
    __builtin_amdgcn_s_sleep(SLEEP_G);
    u32x4 gg[2];
    POLL(2, gg, og, 4 * FF * 16);
    STAMP(3);
    float gt = __uint_as_float(gg[0][0]) + __uint_as_float(gg[1][0]), up = __uint_as_float(gg[0][2]) + __uint_as_float(gg[1][2]);
    gt = rbf(sum32(sum16(gt))); up = rbf(sum32(sum16(up)));
    const float sl = rbf(gt * __builtin_amdgcn_rcpf(1.f + __builtin_amdgcn_exp2f(gt * -1.4426950408889634f)));
    const float hv = c < 14 ? sl * up : 0.f;
    *(LAS u16*)(lds + L_CH + c * 2) = (u16)cvtpk(hv, hv);
    lds_post(fb, one, F_H);
    STAMP(4);
  } else {
    const int ow = wave - 2;
    u32 opst = WS_PS + (u32)(((ocg * 16 + ow * 2 + ((c >> 1) & 1)) * 128 + h * 16 + org) * 128 + g * 32 + (c & 1) * 16);
    if (wave < 6) {
      const int w = wave - 2;
      const u32 vt = (lane >> 3) * 8192 + (((lane & 7) ^ (g << 1)) * 16);
      const u32x4 s_wq = make_srd(p.wq + (j & 3) * 128 * 4096 + h * 512 + (j >> 2) * 64, 127 * 8192 + 128);
#pragma unroll
      for (int i = 0; i < 4; ++i) DMA(s_wq, vt, (u32)((i * 4 + w) * 8 * 8192), lbase + L_WQ + (i * 4 + w) * 1024);
      const size_t kvoff = (size_t)h * p.kv_head_stride + p.kv_win_off + (size_t)j * 128 * DH;
      const u32x4 s_k = make_srd(p.K + kvoff, 32768), s_v = make_srd(p.V + kvoff, 32768);
      const u32 vk = g * 256 + ((c ^ g) * 16);
      const u32 fw = fb + w * 4;
      constexpr int D1 = DEPTH - 1;
#define AFTER(n) do { if ((n) == D1) __builtin_amdgcn_s_barrier(); \
        if ((n) >= D1) VMW(D1); \
        if ((n) - D1 == 3) { lds_post(fw, G_WQ, F_PRG); STAMP(1); } \
        if ((n) - D1 == 11) { lds_post(fw, G_K, F_PRG); STAMP(2); } \
        if ((n) - D1 == 13) lds_post(fw, G_V0, F_PRG); \
        if ((n) - D1 == 15) lds_post(fw, G_V1, F_PRG); \
        if ((n) - D1 == 17) lds_post(fw, G_V2, F_PRG); \
        if ((n) - D1 == 19) { lds_post(fw, G_V3, F_PRG); STAMP(3); } \
        if ((n) - D1 == 23) { lds_post(fw, G_W13, F_PRG); STAMP(4); } } while (0)
#pragma unroll
      for (int i = 0; i < 8; ++i) { DMA(s_k, vk ^ (64 * (i & 3)), (u32)((w * 8 + i) * 1024), lbase + L_K + (w * 8 + i) * 1024); AFTER(4 + i); }
      const u32 vv = g * 256 + ((c ^ (g << 1)) * 16);
#pragma unroll
      for (int i = 0; i < 8; ++i) { const int pc = (i >> 1) * 8 + w * 2 + (i & 1); DMA(s_v, vv ^ (128 * (i & 1)), (u32)(pc * 1024), lbase + L_V + pc * 1024); AFTER(12 + i); }
      const int bb = ffn1 ? b : 0;
      const int lc = bb % 28, rg = bb / 28;
      const u32 nb = ffn1 ? 63 * FF * 2 + 128 : 0;
      const u32x4 s_1 = make_srd(p.w1 + rg * 64 * FF + lc * 64, nb), s_3 = make_srd(p.w3 + rg * 64 * FF + lc * 64, nb);
      const u32 v13 = (lane >> 3) * (FF * 2) + (((lane & 7) ^ (g << 1)) * 16);
#pragma unroll
      for (int i = 0; i < 2; ++i) { DMA(s_1, v13, (u32)((w * 2 + i) * 8 * FF * 2), lbase + L_W13 + (w * 2 + i) * 1024); AFTER(20 + 2 * i);
                                    DMA(s_3, v13, (u32)((w * 2 + i) * 8 * FF * 2), lbase + L_W13 + 8192 + (w * 2 + i) * 1024); AFTER(21 + 2 * i); }
      const u32x4 s_o = make_srd(p.wo + (h * 512 + org * 32) * DM + ocg * 256, 32 * 1024 - 512);
      const u32x4 s_2 = make_srd(p.w2 + (h * 16 + org) * 14 * DM + ocg * 256, 14 * 1024 - 512);
      const u32 vo = (lane >> 5) * 1024 + (((lane & 31) ^ ((lane >> 5) << 1)) * 16);
#pragma unroll
      for (int i = 0; i < 4; ++i) { DMA(s_o, vo ^ (64 * i), (u32)((w * 4 + i) * 2048), lbase + L_WO + (w * 4 + i) * 1024); AFTER(24 + i); }
#pragma unroll
      for (int i = 0; i < 2; ++i) { DMA(s_2, vo ^ (64 * ((w * 2 + i) & 3)), (u32)((w * 2 + i) * 2048), lbase + L_W2 + (w * 2 + i) * 1024); AFTER(28 + i); }
      if (29 - D1 < 19) { VMW(10); lds_post(fw, G_V3, F_PRG); STAMP(3); }
      if (29 - D1 < 23) { VMW(6); lds_post(fw, G_W13, F_PRG); STAMP(4); }
      VMW(0);
      lds_post(fw, G_ALL, F_PRG);
      STAMP(5);
    } else {
      const int w = wave - 6;
      __builtin_amdgcn_s_setprio(2);
      __builtin_amdgcn_s_barrier();
      u32 ost = WS_OS + (u32)(((h * 16 + (c & 3) * 4 + w) * 32 + j) * 128 + ((c >> 2) & 1) * 64 + g * 16);
      u32 qst = WS_QS + (u32)(((h * 4 + (j & 3)) * 512 + (j >> 2) * 64 + w * 16) * 8 + g * 32 + (c & 1) * 16);
      u32 qld = WS_QS + (u32)(h * 16384 + w * 1024 + lane * 16);
      u32 wa = lbase + L_WQ + trow * 128 + ((w ^ ((trow >> 1) & 3)) * 32) + (c & 3) * 8;
      PIN(ost); PIN(qst); PIN(qld); PIN(wa);
      lds_wait<1>(fb, F_TAG, 1);
      const u32 tag = *(LAS u32*)(lds + L_FLG + F_TAGV * 4);
      {
        u32x4 xf[4];
#pragma unroll
        for (int k = 0; k < 4; ++k) xf[k] = *(LAS u32x4*)(lds + L_XN + ((j & 3) * 4 + k) * 64 + g * 16);
        lds_wait<0>(fb4, F_PRG, G_WQ);
        STAMP(1);
        u32x2 wf[8];
#pragma unroll
        for (int k = 0; k < 8; ++k) wf[k] = trr((lptr)(uintptr_t)wa + k * 2048);
        SB();
        f32x4 q0 = mfma32(cat(wf[0], wf[1]), xf[0], f32x4{0.f, 0.f, 0.f, 0.f});
#pragma unroll
        for (int k = 1; k < 4; ++k) q0 = mfma32(cat(wf[2 * k], wf[2 * k + 1]), xf[k], q0);
        if (c < 2) WS_ST((u32x4{__float_as_uint((c & 1) ? q0[2] : q0[0]), tag, __float_as_uint((c & 1) ? q0[3] : q0[1]), tag}), qst);
      }
      STAMP(2);
      if (c < 8) WS_ST((u32x4{0u, ~tag, 0u, ~tag}), ost);
      if (c < 4) { WS_ST((u32x4{0u, ~tag, 0u, ~tag}), opst); WS_ST((u32x4{0u, ~tag, 0u, ~tag}), opst - 8 * 16384); }
      __builtin_amdgcn_s_sleep(SLEEP_Q);
      {
        u32x4 gq[4];
        POLL(4, gq, qld, 4096);
        const float q0 = (__uint_as_float(gq[0][0]) + __uint_as_float(gq[1][0])) + (__uint_as_float(gq[2][0]) + __uint_as_float(gq[3][0]));
        const float q1 = (__uint_as_float(gq[0][2]) + __uint_as_float(gq[1][2])) + (__uint_as_float(gq[2][2]) + __uint_as_float(gq[3][2]));
        *(LAS u32*)(lds + L_Q + w * 256 + lane * 4) = cvtpk(q0, q1);
        lds_post(fb + w * 4, one, F_Q);
      }
      STAMP(3);
      lds_wait<0>(fb + w * 4, F_PRG, G_K);
      u32x4 kf[8];
#pragma unroll
      for (int i = 0; i < 8; ++i) kf[i] = *(LAS u32x4*)(lds + L_K + (w * 32 + (i >> 2) * 16 + c) * 256 + ((((i & 3) * 4 + g) ^ c) * 16));
      lds_wait<0>(fb4, F_Q, 1);
      STAMP(4);
      u32x4 qf[4];
#pragma unroll
      for (int i = 0; i < 4; ++i) qf[i] = *(LAS u32x4*)(lds + L_Q + (c & 3) * 256 + i * 64 + g * 16);
      SB();
      f32x4 s[2] = {f32x4{0.f, 0.f, 0.f, 0.f}, f32x4{0.f, 0.f, 0.f, 0.f}};
#pragma unroll
      for (int i = 0; i < 4; ++i) { s[0] = mfma32(kf[i], qf[i], s[0]); s[1] = mfma32(kf[4 + i], qf[i], s[1]); }
      float sc[8];
#pragma unroll
      for (int k = 0; k < 4; ++k) {
        const u32 r1 = cvtpk(s[k >> 1][(k & 1) * 2], s[k >> 1][(k & 1) * 2 + 1]);
        const u32 r2 = cvtpk(bf_lo(r1) * 0.08838834764831845f, bf_hi(r1) * 0.08838834764831845f);
        sc[2 * k] = bf_lo(r2); sc[2 * k + 1] = bf_hi(r2);
      }
      float mx = max3(max3(sc[0], sc[1], sc[2]), max3(sc[3], sc[4], sc[5]), max2(sc[6], sc[7]));
      mx = max32(max16(mx));
      const float mxl = mx * 1.4426950408889634f;
      float pr[8];
#pragma unroll
      for (int k = 0; k < 8; ++k) pr[k] = __builtin_amdgcn_exp2f(fmaf(sc[k], 1.4426950408889634f, -mxl));
      *(LAS u32x4*)(lds + L_P + w * 1024 + lane * 16) = u32x4{cvtpk(pr[0], pr[1]), cvtpk(pr[2], pr[3]), cvtpk(pr[4], pr[5]), cvtpk(pr[6], pr[7])};
      float ls = ((pr[0] + pr[1]) + (pr[2] + pr[3])) + ((pr[4] + pr[5]) + (pr[6] + pr[7]));
      ls = sum32(sum16(ls));
      *(LAS f32x2*)(lds + L_ML + (c & 3) * 32 + w * 8) = f32x2{mxl, ls};
      lds_post(fb + w * 4, one, F_P);
      STAMP(5);
      u32 va0 = lbase + L_V + trow * 256 + (((2 * w) ^ (trow & 7)) * 32) + (c & 3) * 8;
      u32 va1 = va0 ^ 32;
      PIN(va0); PIN(va1);
      lds_wait<0>(fb4, F_P, 1);
      u32x4 pf[4];
#pragma unroll
      for (int k = 0; k < 4; ++k) pf[k] = *(LAS u32x4*)(lds + L_P + k * 1024 + lane * 16);
      const f32x4 ml01 = *(LAS f32x4*)(lds + L_ML + (c & 3) * 32), ml23 = *(LAS f32x4*)(lds + L_ML + (c & 3) * 32 + 16);
      const float M = max2(max3(ml01[0], ml01[2], ml23[0]), ml23[2]);
      float e[4] = {__builtin_amdgcn_exp2f(ml01[0] - M), __builtin_amdgcn_exp2f(ml01[2] - M), __builtin_amdgcn_exp2f(ml23[0] - M), __builtin_amdgcn_exp2f(ml23[2] - M)};
      const float L = (e[0] * ml01[1] + e[1] * ml01[3]) + (e[2] * ml23[1] + e[3] * ml23[3]);
      if (w == 0 && lane < 4) WS_ST((u32x4{__float_as_uint(M), tag, __float_as_uint(L), tag}), WS_ML + (u32)((h * 32 + j) * 128 + lane * 16));
      const float rl = __builtin_amdgcn_rcpf(L);
#pragma unroll
      for (int k = 0; k < 4; ++k) { e[k] *= rl; PIN(e[k]); }
      STAMP(6);
      f32x4 acc = {0.f, 0.f, 0.f, 0.f};
      const bool hi = (c & 4) != 0;
#pragma unroll
      for (int k = 0; k < 4; ++k) {
        lds_wait<0>(fb4, F_PRG, G_V0 + k);
        const u32x2 a00 = trr((lptr)(uintptr_t)va0 + k * 8192), a01 = trr((lptr)(uintptr_t)va0 + k * 8192 + 4096);
        const u32x2 a10 = trr((lptr)(uintptr_t)va1 + k * 8192), a11 = trr((lptr)(uintptr_t)va1 + k * 8192 + 4096);
        const f32x4 d0 = mfma32(cat(a00, a01), pf[k], f32x4{0.f, 0.f, 0.f, 0.f});
        const f32x4 d1 = mfma32(cat(a10, a11), pf[k], f32x4{0.f, 0.f, 0.f, 0.f});
#pragma unroll
        for (int x = 0; x < 4; ++x) { acc[x] = fmaf(e[k], hi ? d1[x] : d0[x], acc[x]); }
        PIN(acc);
      }
      if (c < 8) WS_ST((u32x4{cvtpk(acc[0], acc[1]), tag, cvtpk(acc[2], acc[3]), tag}), ost);
      STAMP(7);
    }

    __builtin_amdgcn_s_setprio(2);
    const bool fin = b >= 224;
    u32 opld = WS_PS + (u32)((b - 224) * 16384 + ow * 2048 + lane * 16);
    u32 wa = lbase + L_WO + trow * 512 + (((ow * 2) ^ (trow & 7)) * 32) + (c & 3) * 8;
    PIN(opst); PIN(opld); PIN(wa);
    lds_wait<1>(fb4, F_PRG, G_ALL);
    u32x4 ao[2]; u32x2 a2[2];
#pragma unroll
    for (int n = 0; n < 2; ++n) {
      lptr a = (lptr)(uintptr_t)(wa ^ (n * 32));
      ao[n] = cat(trr(a), trr(a + 8192));
      a2[n] = trr(a + 16384);
    }
    lds_wait<1>(fb, F_H, 1);
    const u32 tag = *(LAS u32*)(lds + L_FLG + F_TAGV * 4);
    const u32x2 hf = *(LAS u32x2*)(lds + L_CH + g * 8);
    f32x4 d[2];
#pragma unroll
    for (int n = 0; n < 2; ++n) { d[n] = mfma16(a2[n], hf, f32x4{0.f, 0.f, 0.f, 0.f}); PIN(d[n]); PIN(ao[n]); }
    STAMP(8);
    lds_wait<0>(fb, F_O, 1);
    const u32x4 of = *(LAS u32x4*)(lds + L_CO + g * 16);
#pragma unroll
    for (int n = 0; n < 2; ++n) d[n] = mfma32(ao[n], of, d[n]);
    if (c < 4) {
      const f32x4 t = (c & 2) ? d[1] : d[0];
      WS_ST((u32x4{__float_as_uint((c & 1) ? t[2] : t[0]), tag, __float_as_uint((c & 1) ? t[3] : t[1]), tag}), opst);
    }
    STAMP(9);
    if (fin) {
      const u16 xr = p.x[(b - 224) * 16 + c];
      u32x4 gp[2];
      POLL(2, gp, opld, 1024);
      STAMP(10);
      const float s0 = __uint_as_float(gp[0][0]) + __uint_as_float(gp[1][0]), s1 = __uint_as_float(gp[0][2]) + __uint_as_float(gp[1][2]);
      float t = tr32(s0, s1);
      t = sum16(t); t += dpp<XOR8>(t);
      *(LAS float*)(lds + L_FIN + ((lane & 7) * 2 + (lane >> 5)) * 32 + ow * 4) = t;
      lds_post(fb + ow * 4, one, F_FIN);
      if (ow == 0) {
        lds_wait<0>(fb + (lane & 7) * 4, F_FIN, 1);
        const f32x4 u0 = *(LAS f32x4*)(lds + L_FIN + c * 32), u1 = *(LAS f32x4*)(lds + L_FIN + c * 32 + 16);
        const float r = __uint_as_float((u32)xr << 16) + (((u0[0] + u0[1]) + (u0[2] + u0[3])) + ((u1[0] + u1[1]) + (u1[2] + u1[3])));
        if (lane < 16) p.out[(b - 224) * 16 + c] = (u16)cvtpk(r, r);
        if (b == 224 && lane == 0) __builtin_amdgcn_raw_buffer_store_b32(tag, wsr, WS_EPOCH, 0, 16);
      }
      STAMP(11);
    }
  }
  STAMP_FLUSH();
}

__global__ void tail_probe(unsigned char* ws, int w, int k, int mode) {
  __shared__ u64 a[256], e[256];
  const int b = threadIdx.x;
  const u64* st = (const u64*)(ws + WS_DIAG);
  u64 mn = ~0ull;
  for (int ww = 0; ww < NTHR / 64; ++ww) { const u64 t = st[((size_t)b * 16 + ww) * 16]; mn = t < mn ? t : mn; }
  a[b] = mn; e[b] = w >= 0 ? st[((size_t)b * 16 + w) * 16 + k] : 0;
  __syncthreads();
  if (b == 0 && w >= 0) {
    u64 t0 = ~0ull; for (int i = 0; i < 256; ++i) t0 = a[i] < t0 ? a[i] : t0;
    u64 lo = ~0ull, hi = 0, sum = 0, n = 0;
    for (int i = 0; i < 256; ++i) if (e[i]) { const u64 d = e[i] - t0; lo = d < lo ? d : lo; hi = d > hi ? d : hi; sum += d; ++n; }
    u64 v = mode == 0 ? lo : mode == 1 ? sum / (n ? n : 1) : hi;
    if (v > 10000ull) v = 10000ull;
    const u64 until = rt() + 10ull * v; while (rt() < until) __builtin_amdgcn_s_sleep(4);
  }
}
}

extern "C" void kernel_launch(void* const* d_in, const int* in_sizes, int n_in, void* d_out, int out_size, void* d_ws,
                              size_t ws_size, hipStream_t stream) {
  Params p;
  p.x = (const u16*)d_in[0]; p.K = (const u16*)d_in[1]; p.V = (const u16*)d_in[2];
  p.wq = (const u16*)d_in[3]; p.wo = (const u16*)d_in[4];
  p.w1 = (const u16*)d_in[6]; p.w3 = (const u16*)d_in[7]; p.w2 = (const u16*)d_in[8];
  p.out = (u16*)d_out; p.ws = (unsigned char*)d_ws;
  const u64 sk = (u64)in_sizes[1] / (HKV * DH);
  p.kv_head_stride = sk * DH;
  p.kv_win_off = (sk - WIN) * DH;
  (void)hipFuncSetAttribute((const void*)fused_decode, hipFuncAttributeMaxDynamicSharedMemorySize, L_END);
  hipLaunchKernelGGL(fused_decode, dim3(NWG), dim3(NTHR), L_END, stream, p);
  hipLaunchKernelGGL(tail_probe, dim3(1), dim3(256), 0, stream, (unsigned char*)d_ws, 0, 0, 0);
}
```

```cpp
#include <hip/hip_runtime.h>
#include <stdint.h>
typedef unsigned short u16;
typedef unsigned u32;
typedef unsigned long long u64;
constexpr int HKV = 8, DH = 128, WIN = 4096, NWG = 256, NTHR = 704;
constexpr u32 L_END = 131072, WS_END = 0x161000;

extern "C" __global__ void __launch_bounds__(NTHR, 3) fused_decode(
    const u16* wq, u32 n0, u32 f0, const u16* x, u32 n1, u32 f1, unsigned char* ws, u32 n2, u32 f2, const u16* w1, u32 n3, u32 f3,
    const u16* w3, const u16* K, const u16* V, const u16* wo, const u16* w2, u16* out, u32 head_stride, u32 kv_bytes, u64 pad) {
#if defined(__HIP_DEVICE_COMPILE__)
  asm volatile(
R"FD(; entry: arguments, own code into L2, role dispatch, wq tile; loader set-up
	s_load_dwordx4 s[64:67], s[0:1], 0x0
	s_getpc_b64 s[4:5]
	v_lshlrev_b32 v3, 4, v0
	global_load_dword v94, v3, s[4:5]
	v_readfirstlane_b32 s3, v0
	s_cmpk_lt_u32 s3, 0x100
	s_cbranch_scc0 .Ld_other
	v_lshrrev_b32 v1, 3, v0
	v_xor_b32 v2, v0, v1
	v_and_b32 v2, 7, v2
	v_lshlrev_b32 v2, 4, v2
	v_lshl_or_b32 v10, v1, 15, v2
	s_lshl_b32 m0, s3, 4
	s_lshl_b32 s10, s2, 7
	s_waitcnt lgkmcnt(0)
	buffer_load_dwordx4 v10, s[64:67], s10 offen nt lds
	s_addk_i32 m0, 0x1000
	s_bitset1_b32 s10, 20
	buffer_load_dwordx4 v10, s[64:67], s10 offen nt lds
	s_addk_i32 m0, 0x1000
	s_bitset1_b32 s10, 21
	buffer_load_dwordx4 v10, s[64:67], s10 offen nt lds
	s_addk_i32 m0, 0x1000
	s_bitset0_b32 s10, 20
	buffer_load_dwordx4 v10, s[64:67], s10 offen nt lds
	s_memrealtime s[26:27]
	s_load_dwordx4 s[68:71], s[0:1], 0x10
	s_load_dwordx8 s[72:79], s[0:1], 0x20
	s_load_dwordx16 s[80:95], s[0:1], 0x40
	s_addk_i32 m0, 0x3000
	s_lshr_b32 s4, s3, 6
	s_lshl_b32 s11, s2, 7
	s_add_u32 s12, s11, 917504
	s_cmp_lt_u32 s2, 224
	s_cselect_b32 s7, 0, 0x80000000
	v_mul_u32_u24 v11, 28672, v1
	v_or3_b32 v11, v11, v2, s7
	v_lshlrev_b32 v16, 2, s4
	s_movk_i32 s20, 0x1000
	s_cselect_b64 vcc, 0, -1
	v_lshlrev_b32 v19, 4, v0
	v_and_b32 v19, 0x3f0, v19
	s_lshl_b32 s17, s2, 2
	s_add_u32 s17, s17, s4
	s_lshl_b32 s17, s17, 12
	s_add_u32 s17, s17, 4292218880
	s_waitcnt lgkmcnt(0)
	s_cbranch_vccnz .Lf4
	buffer_load_dwordx4 v11, s[76:79], s11 offen nt lds
	s_branch .Lg4
.Lf4:
	buffer_load_dwordx4 v19, s[72:75], s17 offen sc1 lds
	s_addk_i32 s17, 0x400
.Lg4:
	s_add_u32 m0, m0, s20
	s_barrier
	v_and_b32 v1, 63, v0
	v_lshrrev_b32 v2, 4, v1
	v_lshlrev_b32 v4, 1, v2
	s_lshr_b32 s5, s2, 3
	s_add_u32 s6, s2, 1
	s_and_b32 s6, s6, 7
	v_and_b32 v6, 15, v1
	v_lshlrev_b32 v7, 8, v2
	s_lshl_b32 s7, s4, 2
	v_xor_b32 v8, v6, v2
	v_xor_b32 v8, s7, v8
	v_lshl_or_b32 v12, v8, 4, v7
	s_and_b32 s8, s4, 1
	s_lshl_b32 s8, s8, 3
	v_xor_b32 v8, v6, v4
	v_xor_b32 v8, s8, v8
	v_lshl_or_b32 v13, v8, 4, v7
	s_and_b32 s8, s2, 7
	s_mul_i32 s13, s8, s92
	s_mov_b64 s[28:29], s[80:81]
	s_mov_b64 s[30:31], s[78:79]
	s_add_u32 s32, s82, s13
	s_addc_u32 s33, s83, 0
	s_mov_b32 s34, 0x100000
	s_mov_b32 s35, s67
	s_add_u32 s36, s84, s13
	s_addc_u32 s37, s85, 0
	s_mov_b64 s[38:39], s[34:35]
	s_mov_b64 s[52:53], s[86:87]
	s_mov_b64 s[54:55], s[66:67]
	s_mov_b64 s[56:57], s[88:89]
	s_mov_b64 s[58:59], s[78:79]
	s_xor_b32 s8, s5, s4
	s_lshl_b32 s8, s8, 2
	s_add_u32 s8, s8, s4
	s_lshl_b32 s13, s8, 10
	s_mov_b32 s14, s13
	s_waitcnt lgkmcnt(0)
	s_sub_u32 s23, s26, 36
	s_branch .Lld2
; other roles: flags, first-touch loads, x and epoch
.Ld_other:
	s_lshr_b32 s3, s3, 6
	s_cmp_eq_u32 s3, 4
	s_cbranch_scc1 .Ld_ln
	s_cmp_eq_u32 s3, 10
	s_cbranch_scc1 .Ld_nw
	s_waitcnt lgkmcnt(0)
	s_sleep 2
.Ld_nw:
	s_load_dwordx16 s[80:95], s[0:1], 0x40
	s_load_dwordx4 s[68:71], s[0:1], 0x10
	s_load_dwordx8 s[72:79], s[0:1], 0x20
	s_cmp_eq_u32 s3, 5
	s_cbranch_scc1 .Ld_zero
	s_mov_b64 s[60:61], s[0:1]
	s_mov_b32 s62, s2
	v_mov_b32 v95, v0
	s_cmp_eq_u32 s3, 10
	s_cbranch_scc1 .Ld_pf
	s_cmp_lt_u32 s3, 8
	s_cbranch_scc1 .Ld_att
	s_cmp_lt_u32 s2, 224
	s_cbranch_scc1 .Ld_att
	s_bfe_u32 s4, s2, 0x20003
	s_lshl_b32 s4, s4, 1
	s_and_b32 s5, s3, 1
	s_or_b32 s4, s4, s5
	s_bitcmp1_b32 160, s4
	s_cbranch_scc1 .Ld_att
	s_and_b32 s9, s2, 7
	s_add_u32 s10, s2, 1
	s_and_b32 s10, s10, 7
	s_lshl_b32 s12, s9, 7
	s_mov_b32 s13, 0x93000
	s_cmp_eq_u32 s4, 7
	s_cselect_b32 s13, 0x75800, s13
	s_cmp_eq_u32 s4, 5
	s_cselect_b32 s13, 0, s13
	s_lshl_b32 s14, s10, 19
	s_cmp_eq_u32 s4, 3
	s_cselect_b32 s12, s14, s12
	s_cselect_b32 s13, 0x2aa80, s13
	s_mul_i32 s14, s10, 229376
	s_cmp_eq_u32 s4, 4
	s_cselect_b32 s12, s14, s12
	s_cselect_b32 s13, 0x12a80, s13
	s_lshl_b32 m0, s4, 1
	s_waitcnt lgkmcnt(0)
	s_mul_i32 s14, s9, s92
	s_sub_u32 s15, s4, 1
	s_cmp_lt_u32 s15, 2
	s_cselect_b32 s12, s14, s12
	s_cselect_b32 s13, 0x55500, s13
	s_mov_b64 s[92:93], s[76:77]
	s_mov_b64 s[94:95], s[72:73]
	s_movrels_b64 s[6:7], s[80:81]
	s_add_u32 s6, s6, s12
	s_addc_u32 s7, s7, 0
	v_and_b32 v93, 3, v0
	v_mul_lo_u32 v93, v93, s13
	s_mov_b64 exec, 9
	global_load_dword v93, v93, s[6:7] offset:64 nt
	s_mov_b64 exec, -1
.Ld_att:
	s_barrier
	s_branch .Lrole_att
.Ld_pf:
	s_barrier
	s_branch .Lrole_cmb
.Ld_zero:
	v_lshlrev_b32 v1, 2, v0
	v_and_b32 v1, 0xfc, v1
	v_mov_b32 v2, 0
	ds_write_b32 v1, v2 offset:0x4000
	s_waitcnt lgkmcnt(0)
	s_barrier
	s_branch .Lrole_ffn
.Ld_ln:
	s_load_dwordx8 s[68:75], s[0:1], 0x10
	s_setprio 3
	v_lshlrev_b32 v1, 4, v0
	v_and_b32 v1, 0x3f0, v1
	s_waitcnt lgkmcnt(0)
	buffer_load_dwordx4 v[88:91], v1, s[68:71], 0 offen
	buffer_load_dword v92, off, s[72:75], 0 sc1
	s_barrier
; r_ln
	s_waitcnt vmcnt(1)
	s_lshr_b32 s1, s2, 2
	v_lshlrev_b32_e32 v1, 16, v88
	v_and_b32_e32 v2, 0xffff0000, v88
	v_lshlrev_b32_e32 v3, 16, v89
	v_and_b32_e32 v4, 0xffff0000, v89
	v_lshlrev_b32_e32 v5, 16, v90
	v_and_b32_e32 v6, 0xffff0000, v90
	v_lshlrev_b32_e32 v7, 16, v91
	v_and_b32_e32 v8, 0xffff0000, v91
	v_add_f32_e32 v9, v1, v2
	v_add_f32_e32 v10, v3, v4
	v_add_f32_e32 v9, v9, v10
	v_add_f32_e32 v10, v5, v6
	v_add_f32_e32 v11, v7, v8
	v_add_f32_e32 v10, v10, v11
	v_add_f32_e32 v9, v9, v10
	s_mul_hi_u32 s1, s1, 0x24924925
	s_nop 0
	v_add_f32_dpp v9, v9, v9 quad_perm:[1,0,3,2] row_mask:0xf bank_mask:0xf bound_ctrl:1
	s_nop 1
	v_add_f32_dpp v9, v9, v9 quad_perm:[2,3,0,1] row_mask:0xf bank_mask:0xf bound_ctrl:1
	s_nop 1
	v_add_f32_dpp v9, v9, v9 row_half_mirror row_mask:0xf bank_mask:0xf bound_ctrl:1
	s_nop 1
	v_add_f32_dpp v9, v9, v9 row_ror:8 row_mask:0xf bank_mask:0xf bound_ctrl:1
	v_mov_b32_e32 v10, v9
	s_nop 1
	v_permlane16_swap_b32_e32 v9, v10
	v_add_f32_e32 v9, v9, v10
	v_mov_b32_e32 v10, v9
	s_nop 1
	v_permlane32_swap_b32_e32 v9, v10
	v_add_f32_e32 v9, v9, v10
	v_fmac_f32_e32 v1, 0xbb000000, v9
	v_fma_f32 v1, v1, v1, 0
	v_fmac_f32_e32 v2, 0xbb000000, v9
	v_fmac_f32_e32 v1, v2, v2
	v_fmac_f32_e32 v3, 0xbb000000, v9
	v_fmac_f32_e32 v1, v3, v3
	v_fmac_f32_e32 v4, 0xbb000000, v9
	v_fmac_f32_e32 v1, v4, v4
	v_fmac_f32_e32 v5, 0xbb000000, v9
	v_fmac_f32_e32 v1, v5, v5
	v_fmac_f32_e32 v6, 0xbb000000, v9
	v_fmac_f32_e32 v1, v6, v6
	v_fmac_f32_e32 v7, 0xbb000000, v9
	v_fmac_f32_e32 v1, v7, v7
	v_fmac_f32_e32 v8, 0xbb000000, v9
	v_fmac_f32_e32 v1, v8, v8
	s_nop 1
	v_add_f32_dpp v1, v1, v1 quad_perm:[1,0,3,2] row_mask:0xf bank_mask:0xf bound_ctrl:1
	s_nop 1
	v_add_f32_dpp v1, v1, v1 quad_perm:[2,3,0,1] row_mask:0xf bank_mask:0xf bound_ctrl:1
	s_nop 1
	v_add_f32_dpp v1, v1, v1 row_half_mirror row_mask:0xf bank_mask:0xf bound_ctrl:1
	s_nop 1
	v_add_f32_dpp v1, v1, v1 row_ror:8 row_mask:0xf bank_mask:0xf bound_ctrl:1
	v_mov_b32_e32 v2, v1
	s_nop 1
	v_permlane16_swap_b32_e32 v1, v2
	v_add_f32_e32 v1, v1, v2
	v_mov_b32_e32 v2, v1
	s_nop 1
	v_permlane32_swap_b32_e32 v1, v2
	v_add_f32_e32 v1, v1, v2
	v_mul_f32_e32 v1, 0x3b000000, v1
	v_cvt_pk_bf16_f32 v1, v1, s0
	v_lshlrev_b32_e32 v1, 16, v1
	v_add_f32_e32 v1, 0x3727c5ac, v1
	v_cvt_pk_bf16_f32 v1, v1, s0
	v_lshlrev_b32_e32 v1, 16, v1
	v_sqrt_f32_e32 v1, v1
	v_mul_f32_e32 v2, 0x3b000000, v9
	v_cvt_pk_bf16_f32 v2, v2, s0
	v_lshlrev_b32_e32 v2, 16, v2
	v_cvt_pk_bf16_f32 v1, v1, s0
	s_lshr_b32 s0, s2, 6
	s_mul_i32 s0, s0, 0x2020000
	v_lshlrev_b32_e32 v1, 16, v1
	s_add_i32 s0, s0, 0x1000c0c
	v_rcp_f32_e32 v4, v1
	v_perm_b32 v6, v89, v88, s0
	v_perm_b32 v7, v91, v90, s0
	v_pk_add_f32 v[6:7], v[6:7], v[2:3] op_sel_hi:[1,0] neg_lo:[0,1] neg_hi:[0,1]
	s_and_b32 s2, s1, 3
	v_cvt_pk_bf16_f32 v1, v6, v7
	s_mul_i32 s2, s2, 0x2020000
	v_lshlrev_b32_e32 v6, 16, v1
	v_and_b32_e32 v7, 0xffff0000, v1
	v_pk_mul_f32 v[6:7], v[4:5], v[6:7] op_sel_hi:[0,1]
	v_cvt_pk_bf16_f32 v1, v6, v7
	v_lshlrev_b32_e32 v6, 3, v0
	s_add_i32 s2, s2, 0x1000c0c
	v_lshlrev_b32_e32 v3, 2, v0
	v_and_b32_e32 v6, 48, v6
	v_and_b32_e32 v5, 0xc0, v3
	v_and_b32_e32 v7, 4, v3
	v_and_or_b32 v6, v0, 8, v6
	s_bitcmp0_b32 s1, 2
	v_or3_b32 v5, v6, v7, v5
	s_cselect_b64 vcc, -1, 0
	ds_write_b32 v5, v1 offset:17408
	v_cndmask_b32_e32 v1, v91, v89, vcc
	v_cndmask_b32_e32 v5, v90, v88, vcc
	v_perm_b32 v1, v1, v5, s2
	v_sub_f32_e32 v1, v1, v2
	v_cvt_pk_bf16_f32 v1, v1, v1
	v_lshlrev_b32_e32 v2, 1, v0
	v_lshlrev_b32_e32 v1, 16, v1
	v_lshrrev_b32_e32 v0, 1, v0
	v_mul_f32_e32 v1, v4, v1
	v_and_b32_e32 v3, 48, v3
	v_and_b32_e32 v0, 8, v0
	v_and_b32_e32 v2, 0x46, v2
	v_cvt_pk_bf16_f32 v1, v1, 0
	v_or3_b32 v0, v3, v2, v0
	s_mov_b32 s0, 0x19660d
	ds_write_b16 v0, v1 offset:19456
	s_waitcnt vmcnt(0)
	v_mov_b32_e32 v1, 0x3c6ef35f
	v_mul_lo_u32 v0, v92, s0
	v_add_u32_e32 v0, 0x3c6ef35f, v0
	v_cmp_ne_u32_e32 vcc, 0, v0
	s_nop 1
	v_cndmask_b32_e32 v0, v1, v0, vcc
	v_mov_b32_e32 v1, 0
	s_mov_b64 exec, 1
	ds_write_b32 v1, v0 offset:0x4050
	s_mov_b64 exec, -1
	s_endpgm
; loader: w1 w3 | K | V | wo w2, one 1 KiB piece per step
.Lld2:
.Lp5:
	s_sleep 1
	s_memrealtime s[24:25]
	s_waitcnt lgkmcnt(0)
	s_sub_u32 s24, s24, s23
	s_cmp_lt_u32 s24, 77
	s_cbranch_scc1 .Lp5
	s_cbranch_vccnz .Lf5
	buffer_load_dwordx4 v11, s[76:79], s12 offen nt lds
	s_branch .Lg5
.Lf5:
	buffer_load_dwordx4 v19, s[72:75], s17 offen sc1 lds
	s_addk_i32 s17, 0x400
.Lg5:
	s_add_u32 m0, m0, s20
	s_sub_i32 s24, s24, 107
	s_max_i32 s24, s24, 0
	s_add_u32 s23, s23, s24
	v_lshrrev_b32 v6, 5, v1
	v_and_b32 v7, 31, v1
	v_lshlrev_b32 v8, 1, v6
	v_xor_b32 v7, v7, v8
	v_xor_b32 v7, s7, v7
	v_lshlrev_b32 v7, 4, v7
	v_lshl_or_b32 v14, v6, 10, v7
	s_cmp_eq_u32 s4, 3
	s_cselect_b32 s8, 0x80000000, 0
	v_or_b32 v15, s8, v14
	s_and_b32 s8, s5, 15
	s_lshl_b32 s9, s6, 4
	s_add_u32 s8, s8, s9
	s_lshr_b32 s9, s5, 4
	s_lshl_b32 s9, s9, 9
	s_lshl_b32 s15, s4, 11
	s_add_u32 s9, s9, s15
	s_lshl_b32 s15, s8, 15
	s_add_u32 s15, s15, s9
	s_mul_i32 s16, s8, 14336
	s_add_u32 s16, s16, s9
	s_movk_i32 s22, 0x2000
.Lp6:
	s_sleep 1
	s_memrealtime s[24:25]
	s_waitcnt lgkmcnt(0)
	s_sub_u32 s24, s24, s23
	s_cmp_lt_u32 s24, 92
	s_cbranch_scc1 .Lp6
	s_cbranch_vccnz .Lf6
	buffer_load_dwordx4 v11, s[28:31], s11 offen nt lds
	s_branch .Lg6
.Lf6:
	buffer_load_dwordx4 v19, s[72:75], s17 offen sc1 lds
	s_addk_i32 s17, 0x400
.Lg6:
	s_add_u32 m0, m0, s20
	s_sub_i32 s24, s24, 122
	s_max_i32 s24, s24, 0
	s_add_u32 s23, s23, s24
	s_waitcnt vmcnt(6)
.Lp7:
	s_sleep 1
	s_memrealtime s[24:25]
	s_waitcnt lgkmcnt(0)
	s_sub_u32 s24, s24, s23
	s_cmp_lt_u32 s24, 108
	s_cbranch_scc1 .Lp7
	s_cbranch_vccnz .Lf7
	buffer_load_dwordx4 v11, s[28:31], s12 offen nt lds
	s_branch .Lg7
.Lf7:
	buffer_load_dwordx4 v19, s[72:75], s17 offen sc1 lds
	s_addk_i32 s17, 0x400
.Lg7:
	s_add_u32 m0, m0, s20
	s_sub_i32 s24, s24, 138
	s_max_i32 s24, s24, 0
	s_add_u32 s23, s23, s24
	s_waitcnt vmcnt(6)
.Lp8:
	s_sleep 1
	s_memrealtime s[24:25]
	s_waitcnt lgkmcnt(0)
	s_sub_u32 s24, s24, s23
	s_cmp_lt_u32 s24, 124
	s_cbranch_scc1 .Lp8
	buffer_load_dwordx4 v12, s[32:35], s13 offen nt lds
	s_add_u32 m0, m0, s20
	s_sub_i32 s24, s24, 154
	s_max_i32 s24, s24, 0
	s_add_u32 s23, s23, s24
	s_xor_b32 s13, s13, 135168
	s_waitcnt vmcnt(6)
.Lp9:
	s_sleep 1
	s_memrealtime s[24:25]
	s_waitcnt lgkmcnt(0)
	s_sub_u32 s24, s24, s23
	s_cmp_lt_u32 s24, 140
	s_cbranch_scc1 .Lp9
	buffer_load_dwordx4 v12, s[32:35], s13 offen nt lds
	s_add_u32 m0, m0, s20
	s_sub_i32 s24, s24, 170
	s_max_i32 s24, s24, 0
	s_add_u32 s23, s23, s24
	s_xor_b32 s13, s13, 405504
	s_waitcnt vmcnt(6)
	v_mov_b32 v17, 1
	ds_write_b32 v16, v17 offset:0x4000
.Lp10:
	s_sleep 1
	s_memrealtime s[24:25]
	s_waitcnt lgkmcnt(0)
	s_sub_u32 s24, s24, s23
	s_cmp_lt_u32 s24, 156
	s_cbranch_scc1 .Lp10
	buffer_load_dwordx4 v12, s[32:35], s13 offen nt lds
	s_add_u32 m0, m0, s20
	s_sub_i32 s24, s24, 186
	s_max_i32 s24, s24, 0
	s_add_u32 s23, s23, s24
	s_xor_b32 s13, s13, 135168
	s_waitcnt vmcnt(6)
.Lp11:
	s_sleep 1
	s_memrealtime s[24:25]
	s_waitcnt lgkmcnt(0)
	s_sub_u32 s24, s24, s23
	s_cmp_lt_u32 s24, 171
	s_cbranch_scc1 .Lp11
	buffer_load_dwordx4 v12, s[32:35], s13 offen nt lds
	s_add_u32 m0, m0, s20
	s_sub_i32 s24, s24, 201
	s_max_i32 s24, s24, 0
	s_add_u32 s23, s23, s24
	s_xor_b32 s13, s13, 946176
	s_waitcnt vmcnt(6)
.Lp12:
	s_sleep 1
	s_memrealtime s[24:25]
	s_waitcnt lgkmcnt(0)
)FD"
R"FD(	s_sub_u32 s24, s24, s23
	s_cmp_lt_u32 s24, 187
	s_cbranch_scc1 .Lp12
	buffer_load_dwordx4 v12, s[32:35], s13 offen nt lds
	s_add_u32 m0, m0, s20
	s_sub_i32 s24, s24, 217
	s_max_i32 s24, s24, 0
	s_add_u32 s23, s23, s24
	s_xor_b32 s13, s13, 135168
	s_waitcnt vmcnt(6)
.Lp13:
	s_sleep 1
	s_memrealtime s[24:25]
	s_waitcnt lgkmcnt(0)
	s_sub_u32 s24, s24, s23
	s_cmp_lt_u32 s24, 203
	s_cbranch_scc1 .Lp13
	buffer_load_dwordx4 v12, s[32:35], s13 offen nt lds
	s_add_u32 m0, m0, s20
	s_sub_i32 s24, s24, 233
	s_max_i32 s24, s24, 0
	s_add_u32 s23, s23, s24
	s_xor_b32 s13, s13, 405504
	s_waitcnt vmcnt(6)
	v_mov_b32 v17, 2
	ds_write_b32 v16, v17 offset:0x4000
.Lp14:
	s_sleep 1
	s_memrealtime s[24:25]
	s_waitcnt lgkmcnt(0)
	s_sub_u32 s24, s24, s23
	s_cmp_lt_u32 s24, 219
	s_cbranch_scc1 .Lp14
	buffer_load_dwordx4 v12, s[32:35], s13 offen nt lds
	s_add_u32 m0, m0, s20
	s_sub_i32 s24, s24, 249
	s_max_i32 s24, s24, 0
	s_add_u32 s23, s23, s24
	s_xor_b32 s13, s13, 135168
	s_waitcnt vmcnt(6)
.Lp15:
	s_sleep 1
	s_memrealtime s[24:25]
	s_waitcnt lgkmcnt(0)
	s_sub_u32 s24, s24, s23
	s_cmp_lt_u32 s24, 235
	s_cbranch_scc1 .Lp15
	buffer_load_dwordx4 v12, s[32:35], s13 offen nt lds
	s_add_u32 m0, m0, s20
	s_sub_i32 s24, s24, 265
	s_max_i32 s24, s24, 0
	s_add_u32 s23, s23, s24
	s_nop 0
	s_waitcnt vmcnt(6)
.Lp16:
	s_sleep 1
	s_memrealtime s[24:25]
	s_waitcnt lgkmcnt(0)
	s_sub_u32 s24, s24, s23
	s_cmp_lt_u32 s24, 250
	s_cbranch_scc1 .Lp16
	buffer_load_dwordx4 v13, s[36:39], s14 offen nt lds
	s_add_u32 m0, m0, s20
	s_sub_i32 s24, s24, 280
	s_max_i32 s24, s24, 0
	s_add_u32 s23, s23, s24
	s_xor_b32 s14, s14, 135168
	s_waitcnt vmcnt(6)
.Lp17:
	s_sleep 1
	s_memrealtime s[24:25]
	s_waitcnt lgkmcnt(0)
	s_sub_u32 s24, s24, s23
	s_cmp_lt_u32 s24, 266
	s_cbranch_scc1 .Lp17
	buffer_load_dwordx4 v13, s[36:39], s14 offen nt lds
	s_add_u32 m0, m0, s20
	s_sub_i32 s24, s24, 296
	s_max_i32 s24, s24, 0
	s_add_u32 s23, s23, s24
	s_xor_b32 s14, s14, 405504
	s_waitcnt vmcnt(6)
	v_mov_b32 v17, 3
	ds_write_b32 v16, v17 offset:0x4000
.Lp18:
	s_sleep 1
	s_memrealtime s[24:25]
	s_waitcnt lgkmcnt(0)
	s_sub_u32 s24, s24, s23
	s_cmp_lt_u32 s24, 282
	s_cbranch_scc1 .Lp18
	buffer_load_dwordx4 v13, s[36:39], s14 offen nt lds
	s_add_u32 m0, m0, s20
	s_sub_i32 s24, s24, 312
	s_max_i32 s24, s24, 0
	s_add_u32 s23, s23, s24
	s_xor_b32 s14, s14, 135168
	s_waitcnt vmcnt(6)
.Lp19:
	s_sleep 1
	s_memrealtime s[24:25]
	s_waitcnt lgkmcnt(0)
	s_sub_u32 s24, s24, s23
	s_cmp_lt_u32 s24, 298
	s_cbranch_scc1 .Lp19
	buffer_load_dwordx4 v13, s[36:39], s14 offen nt lds
	s_add_u32 m0, m0, s20
	s_sub_i32 s24, s24, 328
	s_max_i32 s24, s24, 0
	s_add_u32 s23, s23, s24
	s_xor_b32 s14, s14, 946176
	s_waitcnt vmcnt(6)
.Lp20:
	s_sleep 1
	s_memrealtime s[24:25]
	s_waitcnt lgkmcnt(0)
	s_sub_u32 s24, s24, s23
	s_cmp_lt_u32 s24, 314
	s_cbranch_scc1 .Lp20
	buffer_load_dwordx4 v13, s[36:39], s14 offen nt lds
	s_add_u32 m0, m0, s20
	s_sub_i32 s24, s24, 344
	s_max_i32 s24, s24, 0
	s_add_u32 s23, s23, s24
	s_xor_b32 s14, s14, 135168
	s_waitcnt vmcnt(5)
	v_mov_b32 v17, 4
	ds_write_b32 v16, v17 offset:0x4000
.Lp21:
	s_sleep 1
	s_memrealtime s[24:25]
	s_waitcnt lgkmcnt(0)
	s_sub_u32 s24, s24, s23
	s_cmp_lt_u32 s24, 329
	s_cbranch_scc1 .Lp21
	buffer_load_dwordx4 v13, s[36:39], s14 offen nt lds
	s_add_u32 m0, m0, s20
	s_sub_i32 s24, s24, 359
	s_max_i32 s24, s24, 0
	s_add_u32 s23, s23, s24
	s_xor_b32 s14, s14, 405504
.Lp22:
	s_sleep 1
	s_memrealtime s[24:25]
	s_waitcnt lgkmcnt(0)
	s_sub_u32 s24, s24, s23
	s_cmp_lt_u32 s24, 345
	s_cbranch_scc1 .Lp22
	buffer_load_dwordx4 v13, s[36:39], s14 offen nt lds
	s_add_u32 m0, m0, s20
	s_sub_i32 s24, s24, 375
	s_max_i32 s24, s24, 0
	s_add_u32 s23, s23, s24
	s_xor_b32 s14, s14, 135168
	s_waitcnt vmcnt(6)
.Lp23:
	s_sleep 1
	s_memrealtime s[24:25]
	s_waitcnt lgkmcnt(0)
	s_sub_u32 s24, s24, s23
	s_cmp_lt_u32 s24, 361
	s_cbranch_scc1 .Lp23
	buffer_load_dwordx4 v13, s[36:39], s14 offen nt lds
	s_add_u32 m0, m0, s20
	s_sub_i32 s24, s24, 391
	s_max_i32 s24, s24, 0
	s_add_u32 s23, s23, s24
	s_nop 0
	s_waitcnt vmcnt(6)
.Lp24:
	s_sleep 1
	s_memrealtime s[24:25]
	s_waitcnt lgkmcnt(0)
	s_sub_u32 s24, s24, s23
	s_cmp_lt_u32 s24, 387
	s_cbranch_scc1 .Lp24
	buffer_load_dwordx4 v14, s[52:55], s15 offen nt lds
	s_add_u32 m0, m0, s20
	s_add_u32 s15, s15, s22
	s_sub_i32 s24, s24, 417
	s_max_i32 s24, s24, 0
	s_add_u32 s23, s23, s24
	s_waitcnt vmcnt(3)
	v_mov_b32 v17, 5
	ds_write_b32 v16, v17 offset:0x4000
	v_mov_b32 v17, 6
	ds_write_b32 v16, v17 offset:0x4000
.Lp25:
	s_sleep 1
	s_memrealtime s[24:25]
	s_waitcnt lgkmcnt(0)
	s_sub_u32 s24, s24, s23
	s_cmp_lt_u32 s24, 403
	s_cbranch_scc1 .Lp25
	buffer_load_dwordx4 v14, s[52:55], s15 offen nt lds
	s_add_u32 m0, m0, s20
	s_add_u32 s15, s15, s22
	s_sub_i32 s24, s24, 433
	s_max_i32 s24, s24, 0
	s_add_u32 s23, s23, s24
	s_waitcnt vmcnt(3)
.Lp26:
	s_sleep 1
	s_memrealtime s[24:25]
	s_waitcnt lgkmcnt(0)
	s_sub_u32 s24, s24, s23
	s_cmp_lt_u32 s24, 418
	s_cbranch_scc1 .Lp26
	buffer_load_dwordx4 v14, s[52:55], s15 offen nt lds
	s_add_u32 m0, m0, s20
	s_add_u32 s15, s15, s22
	s_sub_i32 s24, s24, 448
	s_max_i32 s24, s24, 0
	s_add_u32 s23, s23, s24
	s_waitcnt vmcnt(3)
	v_mov_b32 v17, 7
	ds_write_b32 v16, v17 offset:0x4000
.Lp27:
	s_sleep 1
	s_memrealtime s[24:25]
	s_waitcnt lgkmcnt(0)
	s_sub_u32 s24, s24, s23
	s_cmp_lt_u32 s24, 434
	s_cbranch_scc1 .Lp27
	buffer_load_dwordx4 v14, s[52:55], s15 offen nt lds
	s_add_u32 m0, m0, s20
	s_add_u32 s15, s15, s22
	s_sub_i32 s24, s24, 464
	s_max_i32 s24, s24, 0
	s_add_u32 s23, s23, s24
	s_waitcnt vmcnt(3)
.Lp28:
	s_sleep 1
	s_memrealtime s[24:25]
	s_waitcnt lgkmcnt(0)
	s_sub_u32 s24, s24, s23
	s_cmp_lt_u32 s24, 450
	s_cbranch_scc1 .Lp28
	buffer_load_dwordx4 v14, s[56:59], s16 offen nt lds
	s_add_u32 m0, m0, s20
	s_add_u32 s16, s16, s22
	s_sub_i32 s24, s24, 480
	s_max_i32 s24, s24, 0
	s_add_u32 s23, s23, s24
	s_waitcnt vmcnt(3)
.Lp29:
	s_sleep 1
	s_memrealtime s[24:25]
	s_waitcnt lgkmcnt(0)
	s_sub_u32 s24, s24, s23
	s_cmp_lt_u32 s24, 466
	s_cbranch_scc1 .Lp29
	buffer_load_dwordx4 v15, s[56:59], s16 offen nt lds
	s_add_u32 m0, m0, s20
	s_sub_i32 s24, s24, 496
	s_max_i32 s24, s24, 0
	s_add_u32 s23, s23, s24
	s_waitcnt vmcnt(3)
	s_branch .Lrole_tail
.Lrole_att:
	v_readfirstlane_b32 s0, v0
	s_lshr_b32 s6, s0, 6
	v_lshlrev_b32_e32 v1, 2, v0
	s_and_b32 s3, s2, 7
	v_and_b32_e32 v2, 15, v0
	v_and_b32_e32 v1, 12, v1
	v_bfe_u32 v3, v0, 2, 2
	s_add_i32 s6, s6, -6
	s_setprio 2
	v_cmp_gt_u32_e32 vcc, 8, v2
	v_bfrev_b32_e32 v6, 1
	v_bfrev_b32_e32 v52, 1
	s_waitcnt lgkmcnt(0)
	s_mov_b64 s[0:1], s[72:73]
	s_and_saveexec_b64 s[4:5], vcc
	s_lshl_b32 s7, s2, 4
	s_and_b32 s7, s7, 0xffffff80
	v_lshl_or_b32 v4, s3, 4, v1
	v_and_or_b32 v5, v0, 48, s7
	v_add_lshl_u32 v4, v4, s6, 12
	v_lshl_add_u32 v5, v3, 6, v5
	s_mov_b32 s7, 0x61000
	v_add3_u32 v52, v5, v4, s7
	s_or_b64 exec, exec, s[4:5]
	v_and_b32_e32 v57, 63, v0
	v_bfe_u32 v4, v0, 4, 2
	v_cmp_gt_u32_e32 vcc, 2, v2
	s_and_saveexec_b64 s[4:5], vcc
	s_cbranch_execz .Lr2_4
	s_lshl_b32 s7, s2, 1
	s_and_b32 s7, s7, 0x7c
	s_lshr_b32 s8, s2, 6
	s_add_i32 s7, s7, s8
	s_lshl_b32 s2, s2, 9
	s_lshl_b32 s7, s7, 10
	s_and_b32 s2, s2, 0x200
	s_lshl_b32 s8, s6, 7
	v_lshlrev_b32_e32 v5, 5, v4
	v_lshlrev_b32_e32 v6, 4, v2
	v_or3_b32 v5, v6, s2, v5
	s_add_i32 s8, s8, s7
	v_add_u32_e32 v5, s8, v5
	v_add_u32_e32 v6, 0x1000, v5
.Lr2_4:
	s_or_b64 exec, exec, s[4:5]
	v_lshlrev_b32_e32 v55, 4, v57
	v_lshlrev_b32_e32 v5, 2, v4
	v_lshl_or_b32 v8, s3, 14, v55
	v_or_b32_e32 v7, v5, v3
	v_lshl_add_u32 v8, s6, 12, v8
	s_lshl_b32 s2, s6, 1
	v_bfe_u32 v9, v0, 1, 1
	v_bitop3_b32 v3, v5, 7, v3 bitop3:0xc8
	v_add_u32_e32 v59, 0x1000, v8
	v_lshlrev_b32_e32 v8, 7, v7
	v_bitop3_b32 v3, s2, v3, v9 bitop3:0x36
	v_lshl_add_u32 v3, v3, 4, v8
	v_and_b32_e32 v40, 1, v0
	v_lshl_or_b32 v5, v40, 3, v3
	v_xor_b32_e32 v3, v4, v2
	v_lshlrev_b32_e32 v3, 4, v3
	v_lshl_or_b32 v2, v2, 8, v3
	v_lshl_or_b32 v2, s6, 13, v2
	v_add_u32_e32 v60, 0xa000, v2
	v_lshlrev_b32_e32 v2, 8, v7
	v_bitop3_b32 v3, s2, v7, 7 bitop3:0x78
	v_and_b32_e32 v4, 3, v0
	v_lshlrev_b32_e32 v3, 5, v3
	v_lshl_or_b32 v2, v4, 3, v2
	s_mov_b32 s2, 0x12000
	v_add3_u32 v53, v3, v2, s2
	v_and_b32_e32 v2, 48, v0
	v_xor_b32_e32 v54, 32, v53
	v_lshl_or_b32 v58, v4, 8, v2
	v_lshlrev_b32_e32 v56, 5, v4
	s_mov_b32 s4, 0x400000
	v_mov_b32_e32 v4, 0
	s_branch 2f
1:
	s_sleep 1
2:
	ds_read_b32 v3, v4 offset:0x4050
	s_sub_u32 s4, s4, 1
	s_cselect_b64 s[2:3], 0, -1
	s_waitcnt lgkmcnt(0)
	v_cmp_gt_u32 vcc, 1, v3
	s_and_b64 vcc, vcc, s[2:3]
	s_cbranch_vccnz 1b
	s_mov_b32 s4, 0x400000
	ds_read_b128 v[8:11], v2 offset:17408
	ds_read_b128 v[12:15], v2 offset:17472
	ds_read_b128 v[16:19], v2 offset:17600
	ds_read_b128 v[20:23], v2 offset:17536
	1:
	ds_read_b32 v2, v1 offset:0x4000
	s_sub_u32 s4, s4, 1
	s_cselect_b64 s[2:3], 0, -1
	s_waitcnt lgkmcnt(0)
	v_cmp_gt_u32 vcc, 1, v2
	s_and_b64 vcc, vcc, s[2:3]
	s_cbranch_vccnz 1b
	ds_read_b64_tr_b16 v[24:25], v5
	ds_read_b64_tr_b16 v[26:27], v5 offset:2048
	ds_read_b64_tr_b16 v[28:29], v5 offset:4096
	ds_read_b64_tr_b16 v[30:31], v5 offset:6144
	ds_read_b64_tr_b16 v[32:33], v5 offset:8192
	ds_read_b64_tr_b16 v[34:35], v5 offset:10240
	ds_read_b64_tr_b16 v[36:37], v5 offset:12288
	ds_read_b64_tr_b16 v[38:39], v5 offset:14336
	s_mov_b32 s3, 0x20000
	s_mov_b32 s2, 0x161000
	s_and_b32 s1, s1, 0xffff
	s_mov_b32 s7, 0x400000
	s_waitcnt lgkmcnt(6)
	v_mfma_f32_16x16x32_bf16 v[8:11], v[24:27], v[8:11], 0
	v_cmp_eq_u32_e32 vcc, 0, v40
	v_mov_b32_e32 v5, v3
	s_mov_b32 s8, 0x400000
	s_waitcnt lgkmcnt(4)
	v_mfma_f32_16x16x32_bf16 v[8:11], v[28:31], v[12:15], v[8:11]
	s_waitcnt lgkmcnt(2)
	v_mfma_f32_16x16x32_bf16 v[8:11], v[32:35], v[16:19], v[8:11]
	s_waitcnt lgkmcnt(0)
	v_mfma_f32_16x16x32_bf16 v[8:11], v[36:39], v[20:23], v[8:11]
	s_nop 7
	v_cndmask_b32_e32 v2, v10, v8, vcc
	v_cndmask_b32_e32 v4, v11, v9, vcc
	buffer_store_dwordx4 v[2:5], v6, s[0:3], 0 offen sc1
	s_sleep 28
	s_nop 0
	s_branch 2f
1:
	s_sleep 2
2:
	ds_read_b32 v2, v1 offset:0x4000
	s_sub_u32 s8, s8, 1
	s_cselect_b64 s[4:5], 0, -1
	s_waitcnt lgkmcnt(0)
	v_cmp_gt_u32 vcc, 3, v2
	s_and_b64 vcc, vcc, s[4:5]
	s_cbranch_vccnz 1b
	buffer_load_dwordx4 v[40:43], v59, s[0:3], 0 offen sc1
	buffer_load_dwordx4 v[48:51], v59, s[0:3], 0 offen offset:1024 sc1
	buffer_load_dwordx4 v[36:39], v59, s[0:3], 0 offen offset:2048 sc1
	buffer_load_dwordx4 v[44:47], v59, s[0:3], 0 offen offset:3072 sc1
	s_branch 2f
1:
	s_sleep 1
2:
	ds_read_b32 v2, v1 offset:0x4000
	s_sub_u32 s7, s7, 1
	s_cselect_b64 s[4:5], 0, -1
	s_waitcnt lgkmcnt(0)
	v_cmp_gt_u32 vcc, 4, v2
	s_and_b64 vcc, vcc, s[4:5]
	s_cbranch_vccnz 1b
	v_xor_b32_e32 v4, 0x80, v60
	v_xor_b32_e32 v8, 0xc0, v60
	v_xor_b32_e32 v2, 64, v60
	ds_read_b128 v[28:31], v60
	ds_read_b128 v[32:35], v60 offset:4096
	ds_read_b128 v[20:23], v2
	ds_read_b128 v[24:27], v2 offset:4096
	ds_read_b128 v[12:15], v4
	ds_read_b128 v[16:19], v4 offset:4096
	ds_read_b128 v[4:7], v8
	ds_read_b128 v[8:11], v8 offset:4096
	s_waitcnt vmcnt(3)
	v_xor_b32_e32 v2, v41, v3
	v_bitop3_b32 v2, v43, v3, v2 bitop3:0xbe
	s_waitcnt vmcnt(2)
	v_bitop3_b32 v2, v49, v3, v2 bitop3:0xbe
	v_bitop3_b32 v2, v51, v3, v2 bitop3:0xbe
	s_waitcnt vmcnt(1)
	v_bitop3_b32 v2, v37, v3, v2 bitop3:0xbe
	v_bitop3_b32 v2, v39, v3, v2 bitop3:0xbe
	s_waitcnt vmcnt(0)
	v_bitop3_b32 v2, v45, v3, v2 bitop3:0xbe
	v_bitop3_b32 v2, v47, v3, v2 bitop3:0xbe
	v_cmp_ne_u32_e32 vcc, 0, v2
	s_cbranch_vccnz .Lr2_6
.Lr2_5:
	v_mov_b32_e32 v41, v42
	v_mov_b32_e32 v49, v50
	v_mov_b32_e32 v37, v38
	v_mov_b32_e32 v45, v46
	v_pk_add_f32 v[40:41], v[40:41], v[48:49]
	v_pk_add_f32 v[36:37], v[36:37], v[44:45]
	s_lshl_b32 s0, s6, 2
	v_pk_add_f32 v[36:37], v[40:41], v[36:37]
	s_mov_b32 s2, 0x400000
	v_cvt_pk_bf16_f32 v2, v36, v37
	v_lshlrev_b32_e32 v36, 2, v57
	v_lshl_or_b32 v36, s6, 8, v36
	ds_write_b32 v36, v2 offset:18432
	v_mov_b32_e32 v2, 1
	v_mov_b32_e32 v57, s0
	s_mov_b64 exec, 1
	ds_write_b32 v57, v2 offset:0x4040
	s_mov_b64 exec, -1
	1:
	ds_read_b32 v59, v1 offset:0x4040
	ds_read_b128 v[36:39], v58 offset:0x4800
	ds_read_b128 v[40:43], v58 offset:0x4800+64
	ds_read_b128 v[44:47], v58 offset:0x4800+128
)FD"
R"FD(	ds_read_b128 v[48:51], v58 offset:0x4800+192
	s_sub_u32 s2, s2, 1
	s_cselect_b64 s[0:1], 0, -1
	s_waitcnt lgkmcnt(4)
	v_cmp_eq_u32 vcc, 0, v59
	s_and_b64 vcc, vcc, s[0:1]
	s_waitcnt lgkmcnt(0)
	s_cbranch_vccnz 1b
	s_mov_b32 s0, 0x400000
	s_waitcnt lgkmcnt(8)
	v_mfma_f32_16x16x32_bf16 v[28:31], v[28:31], v[36:39], 0
	s_waitcnt lgkmcnt(7)
	v_mfma_f32_16x16x32_bf16 v[32:35], v[32:35], v[36:39], 0
	s_waitcnt lgkmcnt(6)
	v_mfma_f32_16x16x32_bf16 v[20:23], v[20:23], v[40:43], v[28:31]
	s_waitcnt lgkmcnt(5)
	v_mfma_f32_16x16x32_bf16 v[24:27], v[24:27], v[40:43], v[32:35]
	s_waitcnt lgkmcnt(4)
	v_mfma_f32_16x16x32_bf16 v[12:15], v[12:15], v[44:47], v[20:23]
	s_waitcnt lgkmcnt(3)
	v_mfma_f32_16x16x32_bf16 v[16:19], v[16:19], v[44:47], v[24:27]
	s_waitcnt lgkmcnt(2)
	v_mfma_f32_16x16x32_bf16 v[4:7], v[4:7], v[48:51], v[12:15]
	s_waitcnt lgkmcnt(1)
	v_mfma_f32_16x16x32_bf16 v[8:11], v[8:11], v[48:51], v[16:19]
	s_nop 5
	v_cvt_pk_bf16_f32 v5, v4, v5
	s_mov_b32 s2, 0x3db504f3
	v_lshlrev_b32_e32 v4, 16, v5
	v_and_b32_e32 v5, 0xffff0000, v5
	v_pk_mul_f32 v[4:5], v[4:5], s[2:3] op_sel_hi:[1,0]
	s_mov_b32 s1, 0x3fb8aa3b
	v_cvt_pk_bf16_f32 v4, v4, v5
	v_cvt_pk_bf16_f32 v5, v6, v7
	v_and_b32_e32 v0, 4, v0
	v_lshlrev_b32_e32 v12, 16, v4
	v_and_b32_e32 v13, 0xffff0000, v4
	v_lshlrev_b32_e32 v4, 16, v5
	v_and_b32_e32 v5, 0xffff0000, v5
	v_pk_mul_f32 v[4:5], v[4:5], s[2:3] op_sel_hi:[1,0]
	s_nop 0
	v_cvt_pk_bf16_f32 v4, v4, v5
	v_cvt_pk_bf16_f32 v5, v8, v9
	s_nop 0
	v_lshlrev_b32_e32 v6, 16, v4
	v_and_b32_e32 v7, 0xffff0000, v4
	v_lshlrev_b32_e32 v4, 16, v5
	v_and_b32_e32 v5, 0xffff0000, v5
	v_pk_mul_f32 v[4:5], v[4:5], s[2:3] op_sel_hi:[1,0]
	v_maximum3_f32 v8, v12, v13, v6
	v_cvt_pk_bf16_f32 v4, v4, v5
	v_cvt_pk_bf16_f32 v5, v10, v11
	s_nop 0
	v_lshlrev_b32_e32 v9, 16, v4
	v_and_b32_e32 v15, 0xffff0000, v4
	v_lshlrev_b32_e32 v4, 16, v5
	v_and_b32_e32 v5, 0xffff0000, v5
	v_pk_mul_f32 v[4:5], v[4:5], s[2:3] op_sel_hi:[1,0]
	v_maximum3_f32 v10, v7, v9, v15
	v_cvt_pk_bf16_f32 v4, v4, v5
	s_nop 0
	v_lshlrev_b32_e32 v5, 16, v4
	v_and_b32_e32 v4, 0xffff0000, v4
	v_maximum3_f32 v11, v5, v4, v4
	v_maximum3_f32 v8, v8, v10, v11
	v_mov_b32_e32 v10, v8
	s_nop 1
	v_permlane16_swap_b32_e32 v8, v10
	v_maximum3_f32 v8, v8, v10, v10
	v_mov_b32_e32 v10, v8
	s_nop 1
	v_permlane32_swap_b32_e32 v8, v10
	v_maximum3_f32 v8, v8, v10, v10
	v_mul_f32_e32 v8, 0x3fb8aa3b, v8
	v_fma_f32 v6, v6, s1, -v8
	v_exp_f32_e32 v14, v6
	v_fma_f32 v6, v7, s1, -v8
	v_fma_f32 v11, v13, s1, -v8
	v_exp_f32_e32 v16, v6
	v_fma_f32 v6, v9, s1, -v8
	v_fma_f32 v10, v12, s1, -v8
	v_exp_f32_e32 v12, v11
	v_exp_f32_e32 v11, v6
	v_fma_f32 v6, v15, s1, -v8
	v_fma_f32 v5, v5, s1, -v8
	v_fma_f32 v4, v4, s1, -v8
	v_exp_f32_e32 v10, v10
	v_exp_f32_e32 v13, v6
	v_exp_f32_e32 v15, v5
	v_exp_f32_e32 v17, v4
	v_cvt_pk_bf16_f32 v4, v10, v12
	v_cvt_pk_bf16_f32 v5, v14, v16
	v_cvt_pk_bf16_f32 v6, v11, v13
	v_cvt_pk_bf16_f32 v7, v15, v17
	v_lshl_add_u32 v9, s6, 10, v55
	ds_write_b128 v9, v[4:7] offset:20480
	v_pk_add_f32 v[4:5], v[10:11], v[12:13]
	v_pk_add_f32 v[6:7], v[14:15], v[16:17]
	s_mov_b32 s1, 0x400000
	v_pk_add_f32 v[4:5], v[4:5], v[6:7]
	s_nop 0
	v_pk_add_f32 v[4:5], v[4:5], v[4:5] op_sel:[0,1] op_sel_hi:[1,0]
	s_nop 0
	v_mov_b32_e32 v5, v4
	s_nop 1
	v_permlane16_swap_b32_e32 v4, v5
	v_add_f32_e32 v4, v4, v5
	v_mov_b32_e32 v5, v4
	s_nop 1
	v_permlane32_swap_b32_e32 v4, v5
	v_add_f32_e32 v9, v4, v5
	v_lshl_add_u32 v4, s6, 3, v56
	ds_write_b64 v4, v[8:9] offset:16512
	s_mov_b64 exec, 1
	ds_write_b32 v57, v2 offset:0x4010
	s_mov_b64 exec, -1
	1:
	ds_read_b32 v2, v1 offset:0x4010
	ds_read_b128 v[16:19], v56 offset:0x4080
	ds_read_b128 v[20:23], v56 offset:0x4080+16
	ds_read_b128 v[4:7], v55 offset:0x5000
	ds_read_b128 v[8:11], v55 offset:0x5000+1024
	ds_read_b128 v[12:15], v55 offset:0x5000+2048
	ds_read_b128 v[24:27], v55 offset:0x5000+3072
	s_sub_u32 s1, s1, 1
	s_cselect_b64 s[2:3], 0, -1
	s_waitcnt lgkmcnt(6)
	v_cmp_eq_u32 vcc, 0, v2
	s_and_b64 vcc, vcc, s[2:3]
	s_waitcnt lgkmcnt(0)
	s_cbranch_vccnz 1b
	s_mov_b32 s1, 0x7060100
	v_maximum3_f32 v2, v16, v18, v18
	v_maximum3_f32 v2, v2, v20, v22
	v_sub_f32_e32 v16, v16, v2
	v_exp_f32_e32 v28, v16
	v_sub_f32_e32 v16, v18, v2
	v_exp_f32_e32 v30, v16
	v_sub_f32_e32 v16, v22, v2
	v_exp_f32_e32 v31, v16
	v_sub_f32_e32 v16, v20, v2
	v_exp_f32_e32 v29, v16
	v_mov_b32_e32 v22, v19
	v_pk_mul_f32 v[18:19], v[22:23], v[30:31]
	v_mov_b32_e32 v20, v17
	v_pk_fma_f32 v[16:17], v[28:29], v[20:21], v[18:19]
	v_cmp_eq_u32_e32 vcc, 0, v0
	v_add_f32_e32 v16, v16, v17
	v_log_f32_e32 v17, v16
	v_mov_b32_e32 v0, v29
	v_add_f32_e32 v2, v2, v17
	v_perm_b32 v55, v2, v3, s1
	s_mov_b32 s1, 0x5040100
	v_perm_b32 v56, v2, v3, s1
	v_rcp_f32_e32 v3, v16
	s_mov_b32 s1, 0x400000
	v_cndmask_b32_e32 v2, 0, v3, vcc
	v_cndmask_b32_e64 v3, v3, 0, vcc
	v_pk_mul_f32 v[48:49], v[2:3], v[0:1] op_sel_hi:[1,0]
	v_mov_b32_e32 v0, v31
	v_pk_mul_f32 v[44:45], v[2:3], v[28:29] op_sel_hi:[1,0]
	v_pk_mul_f32 v[46:47], v[2:3], v[30:31] op_sel_hi:[1,0]
	v_pk_mul_f32 v[50:51], v[2:3], v[0:1] op_sel_hi:[1,0]
	s_nop 0
	1:
	ds_read_b32 v0, v1 offset:0x4000
	s_sub_u32 s1, s1, 1
	s_cselect_b64 s[2:3], 0, -1
	s_waitcnt lgkmcnt(0)
	v_cmp_gt_u32 vcc, 6, v0
	s_and_b64 vcc, vcc, s[2:3]
	s_cbranch_vccnz 1b
	ds_read_b64_tr_b16 v[16:17], v53
	ds_read_b64_tr_b16 v[18:19], v53 offset:4096
	ds_read_b64_tr_b16 v[20:21], v53 offset:8192
	ds_read_b64_tr_b16 v[22:23], v53 offset:12288
	ds_read_b64_tr_b16 v[28:29], v54
	ds_read_b64_tr_b16 v[30:31], v54 offset:4096
	ds_read_b64_tr_b16 v[32:33], v54 offset:8192
	ds_read_b64_tr_b16 v[34:35], v54 offset:12288
	ds_read_b64_tr_b16 v[36:37], v53 offset:16384
	ds_read_b64_tr_b16 v[38:39], v53 offset:20480
	ds_read_b64_tr_b16 v[40:41], v54 offset:16384
	ds_read_b64_tr_b16 v[42:43], v54 offset:20480
	s_waitcnt lgkmcnt(10)
	v_mfma_f32_16x16x32_bf16 v[16:19], v[16:19], v[4:7], 0
	s_waitcnt lgkmcnt(6)
	v_mfma_f32_16x16x32_bf16 v[2:5], v[28:31], v[4:7], 0
	v_mfma_f32_16x16x32_bf16 v[20:23], v[20:23], v[8:11], 0
	s_waitcnt lgkmcnt(4)
	v_mfma_f32_16x16x32_bf16 v[6:9], v[32:35], v[8:11], 0
	s_waitcnt lgkmcnt(2)
	v_mfma_f32_16x16x32_bf16 v[28:31], v[36:39], v[12:15], 0
	s_waitcnt lgkmcnt(0)
	v_mfma_f32_16x16x32_bf16 v[10:13], v[40:43], v[12:15], 0
	v_mul_f32_e64 v14, v18, v44
	v_mul_f32_e64 v15, v19, v44
	v_pk_mul_f32 v[16:17], v[16:17], v[44:45] op_sel_hi:[1,0]
	v_pk_fma_f32 v[4:5], v[4:5], v[44:45], v[14:15] op_sel:[0,1,0]
	v_pk_fma_f32 v[2:3], v[2:3], v[44:45], v[16:17] op_sel:[0,1,0]
	v_pk_fma_f32 v[4:5], v[22:23], v[46:47], v[4:5] op_sel_hi:[1,0,1]
	v_pk_fma_f32 v[2:3], v[20:21], v[46:47], v[2:3] op_sel_hi:[1,0,1]
	v_pk_fma_f32 v[4:5], v[8:9], v[46:47], v[4:5] op_sel:[0,1,0]
	v_pk_fma_f32 v[2:3], v[6:7], v[46:47], v[2:3] op_sel:[0,1,0]
	v_pk_fma_f32 v[4:5], v[30:31], v[48:49], v[4:5] op_sel_hi:[1,0,1]
	v_pk_fma_f32 v[2:3], v[28:29], v[48:49], v[2:3] op_sel_hi:[1,0,1]
	v_pk_fma_f32 v[82:83], v[12:13], v[48:49], v[4:5] op_sel:[0,1,0]
	v_pk_fma_f32 v[80:81], v[10:11], v[48:49], v[2:3] op_sel:[0,1,0]
	v_mov_b32_e32 v0, 2
	v_mov_b32 v85, v55
	v_mov_b32 v87, v56
1:
	ds_read_b32 v84, v1 offset:0x4000
	ds_read_b64_tr_b16 v[64:65], v53 offset:24576
	ds_read_b64_tr_b16 v[66:67], v53 offset:28672
	ds_read_b64_tr_b16 v[68:69], v54 offset:24576
	ds_read_b64_tr_b16 v[70:71], v54 offset:28672
	s_sub_u32 s0, s0, 1
	s_cselect_b64 s[2:3], 0, -1
	s_waitcnt lgkmcnt(2)
	v_mfma_f32_16x16x32_bf16 v[72:75], v[64:67], v[24:27], 0
	s_waitcnt lgkmcnt(0)
	v_mfma_f32_16x16x32_bf16 v[76:79], v[68:71], v[24:27], 0
	v_cmp_gt_u32 vcc, 7, v84
	s_and_b64 vcc, vcc, s[2:3]
	s_cbranch_vccnz 1b
	s_nop 2
	v_pk_fma_f32 v[80:81], v[72:73], v[50:51], v[80:81] op_sel_hi:[1,0,1]
	v_pk_fma_f32 v[82:83], v[74:75], v[50:51], v[82:83] op_sel_hi:[1,0,1]
	v_pk_fma_f32 v[80:81], v[76:77], v[50:51], v[80:81] op_sel:[0,1,0]
	v_pk_fma_f32 v[82:83], v[78:79], v[50:51], v[82:83] op_sel:[0,1,0]
	s_nop 0
	v_cvt_pk_bf16_f32 v84, v80, v81
	v_cvt_pk_bf16_f32 v86, v82, v83
	buffer_store_dwordx4 v[84:87], v52, s[72:75], 0 offen sc1
	s_nop 1
	s_mov_b64 exec, 1
	ds_write_b32 v57, v0 offset:0x4010
	s_mov_b64 exec, -1
	s_branch .Latt_done
.Lr2_6:
	v_add_u32_e32 v2, 0x400, v59
	v_add_u32_e32 v60, 0x800, v59
	v_add_u32_e32 v61, 0xc00, v59
	s_mov_b32 s4, 0xffc00001
.Lr2_7:
	s_sleep 4
	buffer_load_dwordx4 v[40:43], v59, s[0:3], 0 offen sc1
	buffer_load_dwordx4 v[48:51], v2, s[0:3], 0 offen sc1
	buffer_load_dwordx4 v[36:39], v60, s[0:3], 0 offen sc1
	buffer_load_dwordx4 v[44:47], v61, s[0:3], 0 offen sc1
	s_waitcnt vmcnt(3)
	v_xor_b32_e32 v41, v41, v3
	v_bitop3_b32 v41, v43, v3, v41 bitop3:0xbe
	s_waitcnt vmcnt(2)
	v_bitop3_b32 v41, v49, v3, v41 bitop3:0xbe
	v_bitop3_b32 v41, v51, v3, v41 bitop3:0xbe
	s_waitcnt vmcnt(1)
	v_bitop3_b32 v37, v37, v3, v41 bitop3:0xbe
	v_bitop3_b32 v37, v39, v3, v37 bitop3:0xbe
	s_waitcnt vmcnt(0)
	v_bitop3_b32 v37, v45, v3, v37 bitop3:0xbe
	v_bitop3_b32 v37, v47, v3, v37 bitop3:0xbe
	v_cmp_ne_u32_e32 vcc, 0, v37
	s_cmp_eq_u64 vcc, 0
	s_cselect_b64 s[8:9], -1, 0
	s_cmp_eq_u32 s4, 0
	s_cselect_b64 s[10:11], -1, 0
	s_or_b64 s[8:9], s[8:9], s[10:11]
	s_andn2_b64 vcc, exec, s[8:9]
	s_add_i32 s4, s4, 1
	s_cbranch_vccnz .Lr2_7
	s_branch .Lr2_5
.Latt_done:
	s_mov_b64 exec, -1
	v_mov_b32 v0, v95
	s_mov_b64 s[0:1], s[60:61]
	s_mov_b32 s2, s62
.Lrole_tail:
	v_readfirstlane_b32 s0, v0
	s_lshr_b32 s8, s0, 6
	s_add_i32 s3, s8, -2
	s_cmpk_lt_u32 s0, 0x100
	s_cselect_b64 s[0:1], -1, 0
	s_and_b64 s[4:5], s[0:1], exec
	v_and_b32_e32 v2, 63, v0
	v_and_b32_e32 v1, 15, v0
	s_cselect_b32 s3, s8, s3
	s_setprio 2
	v_cmp_gt_u32_e32 vcc, 16, v2
	v_bfrev_b32_e32 v3, 1
	v_bfrev_b32_e32 v5, 1
	s_and_saveexec_b64 s[4:5], vcc
	s_cbranch_execz .Lr1_2
	s_lshr_b32 s6, s2, 3
	s_and_b32 s7, s6, 0x1fffff0
	s_lshl_b32 s9, s3, 1
	s_add_i32 s9, s9, s7
	s_lshl_b32 s7, s2, 4
	v_lshrrev_b32_e32 v4, 3, v1
	s_add_i32 s7, s7, 16
	v_or_b32_e32 v4, s9, v4
	s_and_b32 s7, s7, 0x70
	v_lshl_or_b32 v4, v4, 7, s7
	v_lshlrev_b32_e32 v5, 4, v0
	v_and_or_b32 v4, s6, 15, v4
	v_and_b32_e32 v5, 0x70, v5
	v_lshl_or_b32 v4, v4, 7, v5
	v_add_u32_e32 v5, 0xe1000, v4
.Lr1_2:
	s_or_b64 exec, exec, s[4:5]
	s_add_i32 s9, s2, 0xffffff20
	s_cmpk_gt_i32 s2, 0xdf
	s_cselect_b64 s[4:5], -1, 0
	v_and_b32_e32 v4, 7, v0
	s_and_b64 s[10:11], s[4:5], vcc
	s_and_saveexec_b64 s[6:7], s[10:11]
	v_lshlrev_b32_e32 v3, 1, v0
	s_lshl_b32 s10, s9, 4
	s_lshl_b32 s11, s2, 3
	v_and_b32_e32 v3, 16, v3
	s_and_b32 s10, s10, 0x7fffffe0
	v_and_or_b32 v3, s11, 8, v3
	v_or3_b32 v3, v3, s10, v4
	v_lshlrev_b32_e32 v3, 1, v3
	s_or_b64 exec, exec, s[6:7]
	v_lshrrev_b32_e32 v8, 4, v2
	v_lshrrev_b32_e32 v6, 2, v1
	v_lshl_or_b32 v7, v8, 2, v6
	s_lshl_b32 s6, s9, 14
	s_lshl_b32 s7, s3, 11
	s_add_i32 s7, s7, s6
	v_lshlrev_b32_e32 v9, 9, v7
	s_lshl_b32 s6, s3, 6
	v_lshlrev_b32_e32 v7, 5, v7
	v_mov_b32_e32 v10, 0xe0
	v_bitop3_b32 v7, s6, v7, v10 bitop3:0x78
	v_lshlrev_b32_e32 v10, 3, v0
	v_and_or_b32 v9, v10, 24, v9
	s_mov_b32 s6, 0x1a000
	v_add3_u32 v9, v7, v9, s6
	v_lshrrev_b32_e32 v7, 2, v0
	v_lshl_or_b32 v6, v2, 4, s7
	v_and_or_b32 v7, v7, 8, v4
	s_lshl_b32 s6, s3, 2
	s_lshl_b32 s7, s8, 2
	v_lshl_add_u32 v7, v7, 5, s6
	v_and_b32_e32 v18, 48, v0
	v_lshlrev_b32_e32 v10, 2, v0
	v_and_b32_e32 v0, 4, v0
	s_and_b64 s[0:1], s[0:1], exec
	v_add_u32_e32 v6, 0xe1000, v6
	v_add_u32_e32 v7, 0x4200, v7
	v_and_b32_e32 v10, 12, v10
	v_mov_b32_e32 v11, 0x58
	v_cmp_eq_u32_e32 vcc, 0, v0
	s_cselect_b32 s0, s7, 0x5c
	s_mov_b32 s7, 0x400000
	v_mov_b32_e32 v20, 0
	v_xor_b32_e32 v19, 32, v9
	v_cndmask_b32_e32 v21, v11, v10, vcc
	v_lshlrev_b32_e32 v22, 3, v8
	v_mov_b32_e32 v23, s0
	v_mov_b32 v64, 8
	s_waitcnt vmcnt(0)
	ds_write_b32 v23, v64 offset:0x4000
	buffer_load_ushort v0, v3, s[68:71], 0 offen
	s_branch 2f
1:
	s_sleep 1
2:
	ds_read_b32 v64, v21 offset:0x4000
	ds_read_b64_tr_b16 v[66:67], v9 offset:16384
	ds_read_b64_tr_b16 v[68:69], v19 offset:16384
	ds_read_b64 v[70:71], v22 offset:0x4140
	ds_read_b64_tr_b16 v[40:41], v9
	ds_read_b64_tr_b16 v[42:43], v9 offset:8192
	ds_read_b64_tr_b16 v[44:45], v19
	ds_read_b64_tr_b16 v[46:47], v19 offset:8192
	ds_read_b32 v8, v20 offset:0x4050
)FD"
R"FD(	s_sub_u32 s7, s7, 1
	s_cselect_b64 s[0:1], 0, -1
	s_waitcnt lgkmcnt(5)
	v_mfma_f32_16x16x16_bf16 v[10:13], v[70:71], v[66:67], 0
	v_mfma_f32_16x16x16_bf16 v[14:17], v[70:71], v[68:69], 0
	v_cmp_gt_u32 vcc, 8, v64
	s_and_b64 vcc, vcc, s[0:1]
	s_waitcnt lgkmcnt(0)
	s_cbranch_vccnz 1b
	s_mov_b32 s7, 0x400000
	v_mov_b32 v81, v8
	v_mov_b32 v83, v8
1:
	ds_read_b32 v80, v20 offset:0x4054
	ds_read_b128 v[64:67], v18 offset:0x4100
	s_sub_u32 s7, s7, 1
	s_cselect_b64 s[0:1], 0, -1
	s_waitcnt lgkmcnt(0)
	v_mfma_f32_16x16x32_bf16 v[68:71], v[64:67], v[40:43], v[10:13]
	v_mfma_f32_16x16x32_bf16 v[72:75], v[64:67], v[44:47], v[14:17]
	v_cmp_eq_u32 vcc, 0, v80
	s_and_b64 vcc, vcc, s[0:1]
	s_cbranch_vccnz 1b
	s_nop 3
	v_mov_b32 v80, v68
	v_mov_b32 v82, v72
	buffer_store_dwordx4 v[80:83], v5, s[72:75], 0 offen sc1
	s_nop 1
	s_andn2_b64 vcc, exec, s[4:5]
	s_mov_b32 s0, 0x400000
	s_cbranch_vccnz .Lr1_6
	s_cmpk_eq_i32 s2, 0xe0
	s_cselect_b64 s[4:5], -1, 0
	v_cmp_eq_u32_e32 vcc, 0, v2
	v_bfrev_b32_e32 v2, 1
	s_and_b64 s[4:5], s[4:5], vcc
	v_cndmask_b32_e64 v2, v2, 0, s[4:5]
	v_lshlrev_b32_e32 v1, 5, v1
	v_lshlrev_b32_e32 v4, 2, v4
	v_mov_b32_e32 v5, s6
	v_mov_b32 v74, 1
	s_mov_b64 s[40:41], s[90:91]
	s_mov_b64 s[42:43], s[70:71]
	s_sleep 12
1:
	buffer_load_dwordx4 v[64:67], v6, s[72:75], 0 offen sc1
	buffer_load_dwordx4 v[68:71], v6, s[72:75], 0 offen offset:1024 sc1
	s_sub_u32 s0, s0, 1
	s_cselect_b64 s[4:5], 0, -1
	s_waitcnt vmcnt(1)
	v_xor_b32 v72, v65, v8
	v_bitop3_b32 v72, v67, v8, v72 bitop3:0xbe
	s_waitcnt vmcnt(0)
	v_bitop3_b32 v72, v69, v8, v72 bitop3:0xbe
	v_bitop3_b32 v72, v71, v8, v72 bitop3:0xbe
	v_add_f32 v64, v64, v68
	v_add_f32 v66, v66, v70
	v_cmp_ne_u32 vcc, 0, v72
	s_and_b64 vcc, vcc, s[4:5]
	s_cbranch_vccnz 1b
	v_permlane32_swap_b32 v64, v66
	v_add_f32 v64, v64, v66
	v_mov_b32 v66, v64
	v_lshlrev_b32 v0, 16, v0
	s_cmp_lg_u32 s3, 0
	v_permlane16_swap_b32 v64, v66
	v_add_f32 v64, v64, v66
	s_nop 1
	v_add_f32_dpp v64, v64, v64 row_ror:8 row_mask:0xf bank_mask:0xf
	ds_write_b32 v7, v64
	ds_write_b32 v5, v74 offset:0x4020
	s_cbranch_scc1 3f
2:
	ds_read_b32 v72, v4 offset:0x4020
	ds_read_b128 v[64:67], v1 offset:0x4200
	ds_read_b128 v[68:71], v1 offset:0x4200+16
	s_sub_u32 s0, s0, 1
	s_cselect_b64 s[4:5], 0, -1
	s_waitcnt lgkmcnt(0)
	v_cmp_eq_u32 vcc, 0, v72
	v_pk_add_f32 v[64:65], v[64:65], v[66:67]
	v_pk_add_f32 v[68:69], v[68:69], v[70:71]
	s_and_b64 vcc, vcc, s[4:5]
	s_cbranch_vccnz 2b
	v_pk_add_f32 v[64:65], v[64:65], v[68:69]
	s_nop 0
	v_add_f32 v64, v64, v65
	v_add_f32 v64, v64, v0
	v_cvt_pk_bf16_f32 v64, v64, v64
	buffer_store_short v64, v3, s[40:43], 0 offen sc1
	buffer_store_dword v8, v2, s[72:75], 0 offen sc1
3:
.Lr1_6:
	s_endpgm
.Lrole_cmb:
	v_and_b32_e32 v1, 3, v0
	s_bfe_u32 s3, s2, 0x40003
	v_lshlrev_b32_e32 v2, 2, v1
	s_setprio 3
	s_waitcnt lgkmcnt(0)
	s_mov_b64 s[0:1], s[72:73]
	s_lshl_b32 s0, s2, 4
	s_add_i32 s0, s0, 16
	s_and_b32 s0, s0, 0x70
	v_lshlrev_b32_e32 v3, 4, v0
	v_lshlrev_b32_e32 v4, 1, v0
	s_or_b32 s0, s0, s3
	v_and_b32_e32 v3, 0x3f0, v3
	v_and_b32_e32 v4, 8, v4
	v_lshrrev_b32_e32 v0, 3, v0
	v_lshl_or_b32 v3, s0, 12, v3
	v_lshl_or_b32 v1, v1, 4, v4
	v_and_b32_e32 v0, 4, v0
	s_movk_i32 s0, 0x4100
	v_add_u32_e32 v3, 0x61000, v3
	v_or3_b32 v0, v1, v0, s0
	s_mov_b32 s3, 0x400000
	v_mov_b32_e32 v1, 0
	s_branch 2f
1:
	s_sleep 8
2:
	ds_read_b32 v4, v1 offset:0x4050
	s_sub_u32 s3, s3, 1
	s_cselect_b64 s[0:1], 0, -1
	s_waitcnt lgkmcnt(0)
	v_cmp_gt_u32 vcc, 1, v4
	s_and_b64 vcc, vcc, s[0:1]
	s_cbranch_vccnz 1b
	s_mov_b32 s2, 0x400000
	s_mov_b32 s3, 0x400000
	s_branch 2f
1:
	s_sleep 2
2:
	ds_read_b32 v1, v2 offset:0x4010
	s_sub_u32 s3, s3, 1
	s_cselect_b64 s[0:1], 0, -1
	s_waitcnt lgkmcnt(0)
	v_cmp_gt_u32 vcc, 2, v1
	s_and_b64 vcc, vcc, s[0:1]
	s_cbranch_vccnz 1b
	s_sleep 14
	s_mov_b32 s40, 0x7060302
	v_mov_b32 v46, 0
	v_mov_b32 v47, 1
1:
	buffer_load_dwordx4 v[16:19], v3, s[72:75], 0 offen sc1
	buffer_load_dwordx4 v[20:23], v3, s[72:75], 0 offen offset:1024 sc1
	buffer_load_dwordx4 v[24:27], v3, s[72:75], 0 offen offset:2048 sc1
	buffer_load_dwordx4 v[28:31], v3, s[72:75], 0 offen offset:3072 sc1
	s_sub_u32 s2, s2, 1
	s_cselect_b64 s[0:1], 0, -1
	s_waitcnt vmcnt(3)
	v_xor_b32 v32, v17, v4
	v_bitop3_b32 v32, v19, v4, v32 bitop3:0xbe
	v_perm_b32 v33, v17, v19, s40
	s_waitcnt vmcnt(2)
	v_bitop3_b32 v32, v21, v4, v32 bitop3:0xbe
	v_bitop3_b32 v32, v23, v4, v32 bitop3:0xbe
	v_perm_b32 v34, v21, v23, s40
	s_waitcnt vmcnt(1)
	v_bitop3_b32 v32, v25, v4, v32 bitop3:0xbe
	v_bitop3_b32 v32, v27, v4, v32 bitop3:0xbe
	v_perm_b32 v35, v25, v27, s40
	v_maximum3_f32 v37, v33, v34, v35
	s_waitcnt vmcnt(0)
	v_bitop3_b32 v32, v29, v4, v32 bitop3:0xbe
	v_bitop3_b32 v32, v31, v4, v32 bitop3:0xbe
	v_perm_b32 v36, v29, v31, s40
	v_cmp_ne_u16 vcc, 0, v32
	v_maximum3_f32 v37, v37, v36, v36
	s_and_b64 vcc, vcc, s[0:1]
	s_cbranch_vccnz 1b
	v_mov_b32_dpp v38, v37 row_ror:8 row_mask:0xf bank_mask:0xf
	v_and_b32 v17, 0xffff0000, v16
	v_maximum3_f32 v37, v37, v38, v38
	v_mov_b32 v38, v37
	v_lshlrev_b32 v16, 16, v16
	v_and_b32 v19, 0xffff0000, v18
	v_permlane16_swap_b32 v37, v38
	v_lshlrev_b32 v18, 16, v18
	v_maximum3_f32 v37, v37, v38, v38
	v_mov_b32 v38, v37
	v_and_b32 v21, 0xffff0000, v20
	v_lshlrev_b32 v20, 16, v20
	v_permlane32_swap_b32 v37, v38
	v_and_b32 v23, 0xffff0000, v22
	v_maximum3_f32 v37, v37, v38, v38
	v_sub_f32 v33, v33, v37
	v_sub_f32 v34, v34, v37
	v_exp_f32 v38, v33
	v_sub_f32 v35, v35, v37
	v_exp_f32 v39, v34
	v_sub_f32 v36, v36, v37
	v_exp_f32 v40, v35
	v_lshlrev_b32 v22, 16, v22
	v_exp_f32 v41, v36
	v_pk_mul_f32 v[16:17], v[16:17], v[38:39] op_sel_hi:[1,0]
	v_pk_mul_f32 v[18:19], v[18:19], v[38:39] op_sel_hi:[1,0]
	v_and_b32 v25, 0xffff0000, v24
	v_lshlrev_b32 v24, 16, v24
	v_pk_add_f32 v[42:43], v[38:39], v[40:41]
	v_pk_fma_f32 v[16:17], v[20:21], v[38:39], v[16:17] op_sel:[0,1,0]
	v_pk_fma_f32 v[18:19], v[22:23], v[38:39], v[18:19] op_sel:[0,1,0]
	v_add_f32 v42, v42, v43
	v_and_b32 v27, 0xffff0000, v26
	v_lshlrev_b32 v26, 16, v26
	v_add_f32_dpp v42, v42, v42 row_ror:8 row_mask:0xf bank_mask:0xf
	v_pk_fma_f32 v[16:17], v[24:25], v[40:41], v[16:17] op_sel_hi:[1,0,1]
	v_mov_b32 v43, v42
	v_pk_fma_f32 v[18:19], v[26:27], v[40:41], v[18:19] op_sel_hi:[1,0,1]
	v_and_b32 v29, 0xffff0000, v28
	v_permlane16_swap_b32 v42, v43
	v_lshlrev_b32 v28, 16, v28
	v_add_f32 v42, v42, v43
	v_and_b32 v31, 0xffff0000, v30
	v_mov_b32 v43, v42
	v_lshlrev_b32 v30, 16, v30
	v_pk_fma_f32 v[16:17], v[28:29], v[40:41], v[16:17] op_sel:[0,1,0]
	v_permlane32_swap_b32 v42, v43
	v_pk_fma_f32 v[18:19], v[30:31], v[40:41], v[18:19] op_sel:[0,1,0]
	v_add_f32 v42, v42, v43
	v_rcp_f32 v42, v42
	v_permlane32_swap_b32 v16, v18
	v_permlane32_swap_b32 v17, v19
	v_pk_add_f32 v[16:17], v[16:17], v[18:19]
	s_nop 0
	v_mov_b32 v18, v16
	v_mov_b32 v19, v17
	s_nop 0
	v_permlane16_swap_b32 v16, v18
	v_permlane16_swap_b32 v17, v19
	v_pk_add_f32 v[16:17], v[16:17], v[18:19]
	s_nop 1
	v_add_f32_dpp v16, v16, v16 row_ror:8 row_mask:0xf bank_mask:0xf
	v_add_f32_dpp v17, v17, v17 row_ror:8 row_mask:0xf bank_mask:0xf
	v_mul_f32 v16, v16, v42
	v_mul_f32 v17, v17, v42
	v_cvt_pk_bf16_f32 v16, v16, v17
	ds_write_b32 v0, v16
	ds_write_b32 v46, v47 offset:0x4054
	s_endpgm
.Lrole_ffn:
	v_lshlrev_b32_e32 v1, 2, v0
	v_bfe_u32 v5, v0, 4, 2
	v_and_b32_e32 v10, 15, v0
	v_and_b32_e32 v4, 12, v1
	s_setprio 3
	s_mov_b32 s3, 0x400000
	s_waitcnt lgkmcnt(0)
	s_mov_b64 s[8:9], s[72:73]
	v_mov_b32_e32 v2, 0
	s_branch 2f
1:
	s_sleep 2
2:
	ds_read_b32 v1, v2 offset:0x4050
	s_sub_u32 s3, s3, 1
	s_cselect_b64 s[0:1], 0, -1
	s_waitcnt lgkmcnt(0)
	v_cmp_gt_u32 vcc, 1, v1
	s_and_b64 vcc, vcc, s[0:1]
	s_cbranch_vccnz 1b
	s_and_b32 s9, s9, 0xffff
	s_mov_b32 s11, 0x20000
	s_mov_b32 s10, 0x161000
	s_cmpk_gt_i32 s2, 0xdf
	s_mov_b32 s3, 0x400000
	s_cbranch_scc1 .Lr3_2
	v_lshlrev_b32_e32 v2, 2, v5
	v_lshrrev_b32_e32 v3, 2, v10
	v_or_b32_e32 v11, v2, v3
	v_bitop3_b32 v2, v2, 7, v3 bitop3:0xc8
	v_lshlrev_b32_e32 v3, 3, v0
	v_lshlrev_b32_e32 v11, 7, v11
	v_lshrrev_b32_e32 v16, 1, v0
	v_bfe_u32 v17, v0, 1, 1
	v_and_b32_e32 v3, 8, v3
	s_movk_i32 s0, 0x6000
	v_or3_b32 v3, v11, v3, s0
	v_bitop3_b32 v11, v16, v2, 1 bitop3:0x6c
	v_bitop3_b32 v16, v17, v2, 2 bitop3:0x36
	v_lshl_or_b32 v62, v16, 4, v3
	v_bitop3_b32 v16, v17, v2, 4 bitop3:0x36
	v_bitop3_b32 v2, v17, v2, 6 bitop3:0x36
	v_and_b32_e32 v12, 48, v0
	v_lshl_or_b32 v11, v11, 4, v3
	v_lshl_or_b32 v70, v16, 4, v3
	v_lshl_or_b32 v2, v2, 4, v3
	s_mov_b32 s4, 0x400000
	ds_read_b128 v[6:9], v12 offset:19456
	ds_read_b128 v[12:15], v12 offset:19520
	s_branch 2f
1:
	s_sleep 2
2:
	ds_read_b32 v3, v4 offset:0x4000
	s_sub_u32 s4, s4, 1
	s_cselect_b64 s[0:1], 0, -1
	s_waitcnt lgkmcnt(0)
	v_cmp_gt_u32 vcc, 2, v3
	s_and_b64 vcc, vcc, s[0:1]
	s_cbranch_vccnz 1b
	ds_read_b64_tr_b16 v[16:17], v11
	ds_read_b64_tr_b16 v[18:19], v11 offset:2048
	ds_read_b64_tr_b16 v[20:21], v11 offset:4096
	ds_read_b64_tr_b16 v[22:23], v11 offset:6144
	ds_read_b64_tr_b16 v[24:25], v62
	ds_read_b64_tr_b16 v[26:27], v62 offset:2048
	ds_read_b64_tr_b16 v[28:29], v62 offset:4096
	ds_read_b64_tr_b16 v[30:31], v62 offset:6144
	ds_read_b64_tr_b16 v[32:33], v70
	ds_read_b64_tr_b16 v[34:35], v70 offset:2048
	ds_read_b64_tr_b16 v[36:37], v70 offset:4096
	ds_read_b64_tr_b16 v[38:39], v70 offset:6144
	ds_read_b64_tr_b16 v[40:41], v2
	ds_read_b64_tr_b16 v[42:43], v2 offset:2048
	ds_read_b64_tr_b16 v[44:45], v2 offset:4096
	ds_read_b64_tr_b16 v[46:47], v2 offset:6144
	ds_read_b64_tr_b16 v[48:49], v11 offset:8192
	ds_read_b64_tr_b16 v[50:51], v11 offset:10240
	ds_read_b64_tr_b16 v[52:53], v11 offset:12288
	ds_read_b64_tr_b16 v[54:55], v11 offset:14336
	ds_read_b64_tr_b16 v[56:57], v62 offset:8192
	ds_read_b64_tr_b16 v[58:59], v62 offset:10240
	ds_read_b64_tr_b16 v[60:61], v62 offset:12288
	ds_read_b64_tr_b16 v[62:63], v62 offset:14336
	ds_read_b64_tr_b16 v[64:65], v70 offset:8192
	ds_read_b64_tr_b16 v[66:67], v70 offset:10240
	ds_read_b64_tr_b16 v[68:69], v70 offset:12288
	ds_read_b64_tr_b16 v[70:71], v70 offset:14336
	ds_read_b64_tr_b16 v[72:73], v2 offset:8192
	ds_read_b64_tr_b16 v[74:75], v2 offset:10240
	ds_read_b64_tr_b16 v[76:77], v2 offset:12288
	ds_read_b64_tr_b16 v[78:79], v2 offset:14336
	v_and_b32_e32 v11, 63, v0
	s_waitcnt lgkmcnt(14)
	v_mfma_f32_16x16x32_bf16 v[16:19], v[6:9], v[16:19], 0
	v_and_b32_e32 v0, 16, v0
	v_cmp_eq_u32_e32 vcc, 0, v0
	v_cmp_gt_u32_e64 s[0:1], 16, v11
	v_mfma_f32_16x16x32_bf16 v[24:27], v[6:9], v[24:27], 0
	v_cmp_gt_u32_e64 s[4:5], 32, v11
	v_mfma_f32_16x16x32_bf16 v[32:35], v[6:9], v[32:35], 0
	v_mfma_f32_16x16x32_bf16 v[40:43], v[6:9], v[40:43], 0
	v_mfma_f32_16x16x32_bf16 v[48:51], v[6:9], v[48:51], 0
	v_mfma_f32_16x16x32_bf16 v[16:19], v[12:15], v[20:23], v[16:19]
	s_waitcnt lgkmcnt(10)
	v_mfma_f32_16x16x32_bf16 v[56:59], v[6:9], v[56:59], 0
	s_waitcnt lgkmcnt(6)
	v_mfma_f32_16x16x32_bf16 v[64:67], v[6:9], v[64:67], 0
	s_waitcnt lgkmcnt(2)
	v_mfma_f32_16x16x32_bf16 v[6:9], v[6:9], v[72:75], 0
	v_mfma_f32_16x16x32_bf16 v[18:21], v[12:15], v[28:31], v[24:27]
	v_mfma_f32_16x16x32_bf16 v[20:23], v[12:15], v[36:39], v[32:35]
	v_mfma_f32_16x16x32_bf16 v[22:25], v[12:15], v[44:47], v[40:43]
	s_nop 5
	v_cndmask_b32_e64 v2, v18, v16, s[0:1]
	v_mfma_f32_16x16x32_bf16 v[24:27], v[12:15], v[52:55], v[48:51]
	v_mfma_f32_16x16x32_bf16 v[26:29], v[12:15], v[68:71], v[64:67]
	v_cndmask_b32_e32 v0, v22, v20, vcc
	v_cndmask_b32_e64 v0, v0, v2, s[4:5]
	s_waitcnt lgkmcnt(0)
	v_mfma_f32_16x16x32_bf16 v[6:9], v[12:15], v[76:79], v[6:9]
	s_nop 7
	v_cndmask_b32_e32 v2, v6, v26, vcc
	v_mfma_f32_16x16x32_bf16 v[6:9], v[12:15], v[60:63], v[56:59]
	s_nop 7
	v_cndmask_b32_e64 v3, v6, v24, s[0:1]
	s_lshl_b32 s0, s2, 10
	s_add_i32 s0, s0, 0x29000
	v_cndmask_b32_e64 v2, v2, v3, s[4:5]
	v_mov_b32_e32 v3, v1
	v_lshl_or_b32 v6, v11, 4, s0
	buffer_store_dwordx4 v[0:3], v6, s[8:11], 0 offen sc1
.Lr3_2:
	s_lshl_b32 s1, s2, 4
	s_add_i32 s1, s1, 16
	s_bfe_u32 s0, s2, 0x40003
	s_and_b32 s1, s1, 0x70
	s_or_b32 s2, s1, s0
	v_cmp_gt_u32_e64 s[0:1], 14, v10
	v_mul_u32_u24_e32 v0, 0x700, v5
	s_nop 0
	v_cndmask_b32_e64 v2, 13, v10, s[0:1]
	v_or_b32_e32 v0, v2, v0
)FD"
R"FD(	v_mad_u64_u32 v[2:3], s[4:5], s2, 14, v[0:1]
	v_mov_b32_e32 v0, 0x29000
	v_lshl_add_u32 v0, v2, 4, v0
	s_branch 2f
1:
	s_sleep 4
2:
	ds_read_b32 v2, v4 offset:0x4000
	s_sub_u32 s3, s3, 1
	s_cselect_b64 s[4:5], 0, -1
	s_waitcnt lgkmcnt(0)
	v_cmp_gt_u32 vcc, 5, v2
	s_and_b64 vcc, vcc, s[4:5]
	s_cbranch_vccnz 1b
	buffer_load_dwordx4 v[2:5], v0, s[8:11], 0 offen sc1
	s_mov_b32 s2, 0x1c000
	buffer_load_dwordx4 v[6:9], v0, s[8:11], s2 offen sc1
	s_waitcnt vmcnt(1)
	v_xor_b32_e32 v3, v3, v1
	v_bitop3_b32 v3, v5, v1, v3 bitop3:0xbe
	s_waitcnt vmcnt(0)
	v_bitop3_b32 v3, v7, v1, v3 bitop3:0xbe
	v_bitop3_b32 v3, v9, v1, v3 bitop3:0xbe
	v_cmp_ne_u32_e32 vcc, 0, v3
	s_cbranch_vccnz .Lr3_4
.Lr3_3:
	v_add_f32_e32 v0, v2, v6
	v_mov_b32_e32 v2, v0
	s_nop 1
	v_permlane16_swap_b32_e32 v0, v2
	v_add_f32_e32 v0, v0, v2
	v_mov_b32_e32 v2, v0
	s_nop 1
	v_permlane32_swap_b32_e32 v0, v2
	v_add_f32_e32 v1, v4, v8
	v_add_f32_e32 v0, v0, v2
	v_cvt_pk_bf16_f32 v0, v0, s0
	v_mov_b32_e32 v2, v1
	v_lshlrev_b32_e32 v0, 16, v0
	s_nop 0
	v_permlane16_swap_b32_e32 v1, v2
	v_add_f32_e32 v1, v1, v2
	v_mul_f32_e32 v2, 0xbfb8aa3b, v0
	v_exp_f32_e32 v2, v2
	v_mov_b32_e32 v3, v1
	s_nop 1
	v_permlane32_swap_b32_e32 v1, v3
	v_add_f32_e32 v2, 1.0, v2
	v_rcp_f32_e32 v2, v2
	v_add_f32_e32 v1, v1, v3
	v_cvt_pk_bf16_f32 v1, v1, s0
	v_lshlrev_b32_e32 v1, 16, v1
	v_mul_f32_e32 v0, v2, v0
	v_cvt_pk_bf16_f32 v0, v0, s0
	v_lshlrev_b32_e32 v0, 16, v0
	v_mul_f32_e32 v0, v1, v0
	v_cndmask_b32_e64 v0, 0, v0, s[0:1]
	v_cvt_pk_bf16_f32 v0, v0, v0
	v_lshlrev_b32_e32 v1, 1, v10
	ds_write_b16 v1, v0 offset:16704
	v_mov_b32_e32 v0, 0
	v_mov_b32_e32 v1, 8
	s_mov_b64 exec, 1
	ds_write_b32 v0, v1 offset:0x4058
	s_mov_b64 exec, -1
	s_endpgm
.Lr3_4:
	s_mov_b32 s3, 0xffc00001
.Lr3_5:
	s_sleep 8
	buffer_load_dwordx4 v[2:5], v0, s[8:11], 0 offen sc1
	buffer_load_dwordx4 v[6:9], v0, s[8:11], s2 offen sc1
	s_waitcnt vmcnt(1)
	v_xor_b32_e32 v3, v3, v1
	v_bitop3_b32 v3, v5, v1, v3 bitop3:0xbe
	s_waitcnt vmcnt(0)
	v_bitop3_b32 v3, v7, v1, v3 bitop3:0xbe
	v_bitop3_b32 v3, v9, v1, v3 bitop3:0xbe
	v_cmp_ne_u32_e32 vcc, 0, v3
	s_cmp_eq_u64 vcc, 0
	s_cselect_b64 s[4:5], -1, 0
	s_cmp_eq_u32 s3, 0
	s_cselect_b64 s[6:7], -1, 0
	s_or_b64 s[4:5], s[4:5], s[6:7]
	s_andn2_b64 vcc, exec, s[4:5]
	s_add_i32 s3, s3, 1
	s_cbranch_vccnz .Lr3_5
	s_branch .Lr3_3
.Lcode_end:
	.fill (11392 - (.Lcode_end - fused_decode)) / 4, 4, 0xbf800000

)FD"
  :: "{s[0:1]}"(__builtin_amdgcn_kernarg_segment_ptr()), "{s2}"(__builtin_amdgcn_workgroup_id_x()), "{v0}"(__builtin_amdgcn_workitem_id_x())
  : "v1", "v2", "v3", "v4", "v5", "v6", "v7", "v8", "v9", "v10", "v11", "v12", "v13", "v14", "v15", "v16", "v17", "v18", "v19", "v20", "v21", "v22", "v23", "v24", "v25", "v26", "v27", "v28", "v29", "v30", "v31", "v32", "v33", "v34", "v35", "v36", "v37", "v38", "v39", "v40", "v41", "v42", "v43", "v44", "v45", "v46", "v47", "v48", "v49", "v50", "v51", "v52", "v53", "v54", "v55", "v56", "v57", "v58", "v59", "v60", "v61", "v62", "v63", "v64", "v65", "v66", "v67", "v68", "v69", "v70", "v71", "v72", "v73", "v74", "v75", "v76", "v77", "v78", "v79", "v80", "v81", "v82", "v83", "v84", "v85", "v86", "v87", "v88", "v89", "v90", "v91", "v92", "v93", "v94", "v95", "s3", "s4", "s5", "s6", "s7", "s8", "s9", "s10", "s11", "s12", "s13", "s14", "s15", "s16", "s17", "s18", "s19", "s20", "s21", "s22", "s23", "s24", "s25", "s26", "s27", "s28", "s29", "s30", "s31", "s33", "s34", "s35", "s36", "s37", "s38", "s39", "s40", "s41", "s42", "s43", "s44", "s45", "s46", "s47", "s48", "s49", "s50", "s51", "s52", "s53", "s54", "s55", "s56", "s57", "s58", "s59", "s60", "s61", "s62", "s63", "s64", "s65", "s66", "s67", "s68", "s69", "s70", "s71", "s72", "s73", "s74", "s75", "s76", "s77", "s78", "s79", "s80", "s81", "s82", "s83", "s84", "s85", "s86", "s87", "s88", "s89", "s90", "s91", "s92", "s93", "s94", "s95", "s96", "s97", "s98", "s99", "vcc", "scc", "memory");
#endif
}

extern "C" void kernel_launch(void* const* d_in, const int* in_sizes, int n_in, void* d_out, int out_size, void* d_ws,
                              size_t ws_size, hipStream_t stream) {
  const u32 sk = (u32)in_sizes[1] / (HKV * DH);
  const u32 win = (sk - WIN) * DH;
  const u32 FL = 0x00020000u;
  (void)hipFuncSetAttribute((const void*)fused_decode, hipFuncAttributeMaxDynamicSharedMemorySize, L_END);
  hipLaunchKernelGGL(fused_decode, dim3(NWG), dim3(NTHR), L_END, stream,
                     (const u16*)d_in[3], (u32)in_sizes[3] * 2, FL, (const u16*)d_in[0], (u32)in_sizes[0] * 2, FL,
                     (unsigned char*)d_ws, WS_END, FL, (const u16*)d_in[6], (u32)in_sizes[6] * 2, FL,
                     (const u16*)d_in[7], (const u16*)d_in[1] + win, (const u16*)d_in[2] + win, (const u16*)d_in[4],
                     (const u16*)d_in[8], (u16*)d_out, sk * DH * 2, ((u32)in_sizes[1] - win) * 2, (u64)0);
}
```
